```python
import torch

def _softmax(x, axis=-1):
    return torch.softmax(x, dim=axis)

def _gelu(x):
    return torch.nn.functional.gelu(x, approximate='tanh')

def _silu(x):
    return torch.nn.functional.silu(x)

def _sigmoid(x):
    return torch.sigmoid(x)

def _layernorm(x, axis=-1, eps=1e-05):
    m = x.mean(dim=axis, keepdim=True)
    v = x.var(dim=axis, keepdim=True, unbiased=False)
    return (x - m) / torch.sqrt(v + eps)

def _scatter_add(tgt, idx, src, axis=0):
    return tgt.clone().index_add_(axis, idx, src)

def _segment_sum(src, seg, n):
    out = torch.zeros((n,) + src.shape[1:], dtype=src.dtype)
    return out.index_add_(0, seg, src)

def _pad(x, pw):
    flat = tuple((v for pair in reversed(pw) for v in pair))
    return torch.nn.functional.pad(x, flat)
kernel_name = 'standalone__tree_speculative_verify_tree_attention_over_candidate_token_tree__decode'

def setup_inputs():

    _w = lambda a: (lambda t: t.to(torch.bfloat16) if t.is_floating_point() else t)(torch.as_tensor(a))
    B, Hq, Hkv, T, C, D = (2, 32, 16, 1, 8192, 128)
    tree_mask = torch.zeros((B, T, T), dtype=float)
    for b in range(B):
        parent = torch.zeros(T, dtype=torch.int64)
        for i in range(1, T):
            parent[i] = int(torch.randint(0, i, (1,)).item())
        for i in range(T):
            j = i
            tree_mask[b, i, j] = 1.0
            while j > 0:
                j = parent[j]
                tree_mask[b, i, j] = 1.0
    return {'q': _w((lambda *s: torch.randn(*s, dtype=torch.bfloat16) * 0.8)(B, Hq, T, D)), 'k_cache': _w((lambda *s: torch.randn(*s, dtype=torch.bfloat16) * 0.8)(B, Hkv, C, D)), 'v_cache': _w((lambda *s: torch.randn(*s, dtype=torch.bfloat16) * 0.05)(B, Hkv, C, D)), 'k_tree': _w((lambda *s: torch.randn(*s, dtype=torch.bfloat16) * 0.8)(B, Hkv, T, D)), 'v_tree': _w((lambda *s: torch.randn(*s, dtype=torch.bfloat16) * 0.05)(B, Hkv, T, D)), 'tree_mask': _w(tree_mask)}

def reference(q, k_cache, v_cache, k_tree, v_tree, tree_mask):
    B, Hq, T, D = q.shape
    Hkv, C = (k_cache.shape[1], k_cache.shape[2])
    rep = Hq // Hkv
    kc = (lambda x, n, axis: torch.repeat_interleave(x, n, dim=axis))(k_cache, rep, axis=1)
    vc = (lambda x, n, axis: torch.repeat_interleave(x, n, dim=axis))(v_cache, rep, axis=1)
    kt = (lambda x, n, axis: torch.repeat_interleave(x, n, dim=axis))(k_tree, rep, axis=1)
    vt = (lambda x, n, axis: torch.repeat_interleave(x, n, dim=axis))(v_tree, rep, axis=1)
    k = torch.concatenate([kc, kt], axis=2)
    v = torch.concatenate([vc, vt], axis=2)
    scale = D ** (-0.5)
    scores = torch.einsum('bhqd,bhkd->bhqk', q, k) * scale
    ctx_mask = torch.ones((B, 1, T, C), dtype=scores.dtype)
    tm = tree_mask[:, None, :, :].clone()
    full_mask = torch.concatenate([ctx_mask, tm], axis=-1)
    scores = scores + (1.0 - full_mask) * -1000000000.0
    p = _softmax(scores, axis=-1)
    return torch.einsum('bhqk,bhkd->bhqd', p, v)
```

```cpp
#include <hip/hip_runtime.h>
#include <hip/hip_bf16.h>
#include <stdint.h>

typedef __attribute__((ext_vector_type(8))) __bf16 bf16x8;
typedef __attribute__((ext_vector_type(4))) float f32x4;
typedef __attribute__((ext_vector_type(4))) unsigned u32x4;

constexpr int NB = 2, HQ = 32, HKV = 16, CTX = 8192, HD = 128;
constexpr int NGRP = NB * HKV;
constexpr int NSPLIT = 8, WAVES = 8;
constexpr int TPW = CTX / NSPLIT / WAVES;
constexpr int NT = TPW / 16;
constexpr float C2 = 0.088388347648318440f * 1.4426950408889634f;

__device__ __forceinline__ u32x4 ldnt(const void* p) { return __builtin_nontemporal_load(reinterpret_cast<const u32x4*>(p)); }
__device__ __forceinline__ float bf2f(unsigned short x) { return __uint_as_float((unsigned)x << 16); }
__device__ __forceinline__ unsigned cvtpk(float lo, float hi) {
  typedef __attribute__((ext_vector_type(2))) __bf16 bf16x2;
  typedef __attribute__((ext_vector_type(2))) float f32x2;
  f32x2 v = {lo, hi};
  bf16x2 r = __builtin_convertvector(v, bf16x2);
  return __builtin_bit_cast(unsigned, r);
}
__device__ __forceinline__ float xh_max(float x) {
  auto s = __builtin_amdgcn_permlane16_swap(__float_as_uint(x), __float_as_uint(x), false, false);
  x = fmaxf(__uint_as_float(s[0]), __uint_as_float(s[1]));
  auto t = __builtin_amdgcn_permlane32_swap(__float_as_uint(x), __float_as_uint(x), false, false);
  return fmaxf(__uint_as_float(t[0]), __uint_as_float(t[1]));
}
__device__ __forceinline__ float xh_sum(float x) {
  auto s = __builtin_amdgcn_permlane16_swap(__float_as_uint(x), __float_as_uint(x), false, false);
  x = __uint_as_float(s[0]) + __uint_as_float(s[1]);
  auto t = __builtin_amdgcn_permlane32_swap(__float_as_uint(x), __float_as_uint(x), false, false);
  return __uint_as_float(t[0]) + __uint_as_float(t[1]);
}

__global__ void __launch_bounds__(512, 2) attn_split(const unsigned short* __restrict__ q, const unsigned short* __restrict__ kc,
                                                     const unsigned short* __restrict__ vc, float* __restrict__ ws) {
  __shared__ __attribute__((aligned(16))) float lds[WAVES * 2 * 132];
  const int bid = blockIdx.x, g = bid >> 3, s = bid & 7;
  const int tid = threadIdx.x, lane = tid & 63, wave = __builtin_amdgcn_readfirstlane(tid >> 6);
  const int r = lane & 15, h = lane >> 4;
  const size_t tok0 = (size_t)g * CTX + s * (CTX / NSPLIT) + wave * TPW;
  const char* kb = (const char*)kc + tok0 * 256 + (4 * (r & 3) + (r >> 2)) * 256 + h * 16;
  const char* vb = (const char*)vc + tok0 * 256 + lane * 16;
  u32x4 buf[32];
#pragma unroll
  for (int t = 0; t < NT; ++t)
#pragma unroll
    for (int c = 0; c < 4; ++c) buf[t * 4 + c] = ldnt(kb + t * 4096 + c * 64);
  u32x4 qf[4];
  const char* qb = (const char*)q + ((size_t)g * 2 + (r >> 3)) * 256 + h * 16;
#pragma unroll
  for (int c = 0; c < 4; ++c) qf[c] = *reinterpret_cast<const u32x4*>(qb + c * 64);
  f32x4 S[NT];
#pragma unroll
  for (int t = 0; t < NT; ++t) {
    f32x4 a = {0.f, 0.f, 0.f, 0.f};
#pragma unroll
    for (int c = 0; c < 4; ++c)
      a = __builtin_amdgcn_mfma_f32_16x16x32_bf16(__builtin_bit_cast(bf16x8, buf[t * 4 + c]), __builtin_bit_cast(bf16x8, qf[c]), a, 0, 0, 0);
    S[t] = a;
    __builtin_amdgcn_sched_barrier(0);
#pragma unroll
    for (int c = 0; c < 4; ++c) buf[t * 4 + c] = ldnt(vb + t * 4096 + c * 1024);
    __builtin_amdgcn_sched_barrier(0);
  }
  float m = S[0][0];
#pragma unroll
  for (int t = 0; t < NT; ++t)
#pragma unroll
    for (int c = 0; c < 4; ++c) m = fmaxf(m, S[t][c]);
  m = xh_max(m);
  const float mc = m * C2;
  float l = 0.f;
  unsigned msk[4];
#pragma unroll
  for (int c = 0; c < 4; ++c) msk[c] = (c == ((r & 7) >> 1)) ? ((r & 1) ? 0xFFFF0000u : 0x0000FFFFu) : 0u;
  f32x4 acc = {0.f, 0.f, 0.f, 0.f};
#pragma unroll
  for (int t = 0; t < NT; ++t)
#pragma unroll
    for (int c = 0; c < 4; ++c) {
      const float p = __builtin_amdgcn_exp2f(fmaf(S[t][c], C2, -mc));
      l += p;
      const unsigned pp = cvtpk(p, p);
      u32x4 a = {pp & msk[0], pp & msk[1], pp & msk[2], pp & msk[3]};
      acc = __builtin_amdgcn_mfma_f32_16x16x32_bf16(__builtin_bit_cast(bf16x8, a), __builtin_bit_cast(bf16x8, buf[t * 4 + c]), acc, 0, 0, 0);
    }
  l = xh_sum(l);
  float* rec = lds + (wave * 2 + (lane >> 5)) * 132;
  *reinterpret_cast<f32x4*>(rec + 4 + 8 * r + 4 * (h & 1)) = acc;
  if (h == 0 && (r & 7) == 0) { float* rr = lds + (wave * 2 + (r >> 3)) * 132; rr[0] = mc; rr[1] = l; }
  __syncthreads();
  if (tid < 256) {
    const int hd = tid >> 7, d = tid & 127;
    float M = -INFINITY;
#pragma unroll
    for (int w = 0; w < WAVES; ++w) M = fmaxf(M, lds[(w * 2 + hd) * 132]);
    float L = 0.f, o = 0.f;
#pragma unroll
    for (int w = 0; w < WAVES; ++w) {
      const float* rr = lds + (w * 2 + hd) * 132;
      const float wt = __builtin_amdgcn_exp2f(rr[0] - M);
      L = fmaf(wt, rr[1], L); o = fmaf(wt, rr[4 + d], o);
    }
    float* po = ws + ((size_t)(g * 2 + hd) * NSPLIT + s) * 132;
    po[4 + d] = o;
    if (d == 0) { po[0] = M; po[1] = L; }
  }
}

__global__ void __launch_bounds__(128) attn_combine(const unsigned short* __restrict__ q, const unsigned short* __restrict__ kt,
                                                    const unsigned short* __restrict__ vt, const unsigned short* __restrict__ mask,
                                                    const float* __restrict__ ws, unsigned short* __restrict__ out) {
  __shared__ float red[2];
  const int row = blockIdx.x, d = threadIdx.x, g = row >> 1, b = g / HKV;
  float x = bf2f(q[row * HD + d]) * bf2f(kt[g * HD + d]);
#pragma unroll
  for (int o = 32; o >= 1; o >>= 1) x += __shfl_xor(x, o);
  if ((d & 63) == 0) red[d >> 6] = x;
  __syncthreads();
  const float st = ((red[0] + red[1]) * 0.088388347648318440f + (1.0f - bf2f(mask[b])) * -1000000000.0f) * 1.4426950408889634f;
  const float* pw = ws + (size_t)row * NSPLIT * 132;
  float M = st;
#pragma unroll
  for (int s = 0; s < NSPLIT; ++s) M = fmaxf(M, pw[s * 132]);
  float wt = __builtin_amdgcn_exp2f(st - M);
  float L = wt, o = wt * bf2f(vt[g * HD + d]);
#pragma unroll
  for (int s = 0; s < NSPLIT; ++s) {
    wt = __builtin_amdgcn_exp2f(pw[s * 132] - M);
    L = fmaf(wt, pw[s * 132 + 1], L); o = fmaf(wt, pw[s * 132 + 4 + d], o);
  }
  out[row * HD + d] = (unsigned short)(cvtpk(o / L, 0.f) & 0xFFFFu);
}

extern "C" void kernel_launch(void* const* d_in, const int* in_sizes, int n_in, void* d_out, int out_size, void* d_ws, size_t ws_size,
                              hipStream_t stream) {
  const unsigned short* q = (const unsigned short*)d_in[0];
  const unsigned short* kc = (const unsigned short*)d_in[1];
  const unsigned short* vc = (const unsigned short*)d_in[2];
  const unsigned short* kt = (const unsigned short*)d_in[3];
  const unsigned short* vt = (const unsigned short*)d_in[4];
  const unsigned short* mask = (const unsigned short*)d_in[5];
  attn_split<<<NGRP * NSPLIT, 512, 0, stream>>>(q, kc, vc, (float*)d_ws);
  attn_combine<<<NB * HQ, 128, 0, stream>>>(q, kt, vt, mask, (const float*)d_ws, (unsigned short*)d_out);
}
```

```cpp
#include <hip/hip_runtime.h>
#include <hip/hip_bf16.h>
#include <stdint.h>

#define AUXK 2
#define AUXV 2

typedef __attribute__((ext_vector_type(8))) __bf16 bf16x8;
typedef __attribute__((ext_vector_type(4))) float f32x4;
typedef __attribute__((ext_vector_type(4))) unsigned u32x4;
typedef __attribute__((address_space(1))) unsigned long long gu64;
typedef __attribute__((address_space(1))) unsigned gu32;

constexpr int NB = 2, HQ = 32, HKV = 16, CTX = 8192, HD = 128;
constexpr int NGRP = NB * HKV;
constexpr int NSPLIT = 8, WAVES = 16;
constexpr int TPW = CTX / NSPLIT / WAVES;
constexpr int NU = TPW / 4;
constexpr float SCALE = 0.088388347648318440f, LOG2E = 1.4426950408889634f;
constexpr float C2 = SCALE * LOG2E;
constexpr int REC = 132;
constexpr int GR = 136;
constexpr size_t WS_GRAN = 65536;

__device__ __forceinline__ float bf2f(unsigned short x) { return __uint_as_float((unsigned)x << 16); }
__device__ __forceinline__ unsigned cvtpk(float lo, float hi) {
  typedef __attribute__((ext_vector_type(2))) __bf16 bf16x2;
  typedef __attribute__((ext_vector_type(2))) float f32x2;
  f32x2 v = {lo, hi};
  bf16x2 r = __builtin_convertvector(v, bf16x2);
  return __builtin_bit_cast(unsigned, r);
}
template <int CTRL> __device__ __forceinline__ float dpp(float x) {
  return __builtin_bit_cast(float, __builtin_amdgcn_mov_dpp(__builtin_bit_cast(int, x), CTRL, 0xf, 0xf, true));
}
__device__ __forceinline__ float xh_max(float x) {
  auto s = __builtin_amdgcn_permlane16_swap(__float_as_uint(x), __float_as_uint(x), false, false);
  x = fmaxf(__uint_as_float(s[0]), __uint_as_float(s[1]));
  auto t = __builtin_amdgcn_permlane32_swap(__float_as_uint(x), __float_as_uint(x), false, false);
  return fmaxf(__uint_as_float(t[0]), __uint_as_float(t[1]));
}
__device__ __forceinline__ float xh_sum(float x) {
  auto s = __builtin_amdgcn_permlane16_swap(__float_as_uint(x), __float_as_uint(x), false, false);
  x = __uint_as_float(s[0]) + __uint_as_float(s[1]);
  auto t = __builtin_amdgcn_permlane32_swap(__float_as_uint(x), __float_as_uint(x), false, false);
  return __uint_as_float(t[0]) + __uint_as_float(t[1]);
}
#define RLX_AGENT __ATOMIC_RELAXED, __HIP_MEMORY_SCOPE_AGENT

__global__ void __launch_bounds__(WAVES * 64, 4)
tree_attn_decode(const unsigned short* __restrict__ q, const unsigned short* __restrict__ kc, const unsigned short* __restrict__ vc,
                 const unsigned short* __restrict__ kt, const unsigned short* __restrict__ vt, const unsigned short* __restrict__ mask,
                 unsigned short* __restrict__ out, unsigned char* ws) {
  __shared__ __attribute__((aligned(16))) float lds[(WAVES + 1) * 2 * REC];
  const int bid = blockIdx.x, g = bid >> 3, s = bid & 7;
  const int tid = threadIdx.x, lane = tid & 63, wave = __builtin_amdgcn_readfirstlane(tid >> 6);
  const int r = lane & 15, h = lane >> 4;
  const __amdgpu_buffer_rsrc_t rk = __builtin_amdgcn_make_buffer_rsrc((void*)kc, 0, NGRP * CTX * HD * 2, 0x00020000);
  const __amdgpu_buffer_rsrc_t rv = __builtin_amdgcn_make_buffer_rsrc((void*)vc, 0, NGRP * CTX * HD * 2, 0x00020000);
  const u32x4 qf = *reinterpret_cast<const u32x4*>((const char*)q + (g * 2 + ((r >> 2) & 1)) * 256 + (4 * h + (r & 3)) * 16);
  gu32* ep = (gu32*)(ws + g * 256);
  const unsigned tag = __hip_atomic_load(ep, RLX_AGENT) + 1u;
  __builtin_amdgcn_sched_barrier(0);
  const unsigned wbase = (unsigned)(bid * WAVES + wave) * (TPW * 256);
  const unsigned rot = (unsigned)(bid * WAVES + wave);
  const unsigned kvo = (r >> 2) * 256 + h * 64 + (r & 3) * 16, vvo = lane * 16;
  u32x4 buf[NU];
#pragma unroll
  for (int u = 0; u < NU; ++u)
    buf[u] = __builtin_amdgcn_raw_buffer_load_b128(rk, kvo + (u & 3) * 1024, wbase + (((u >> 2) + rot) & 3) * 4096, AUXK);
  __builtin_amdgcn_sched_barrier(0);
  float S[NU];
#pragma unroll
  for (int u = 0; u < NU; ++u) {
    const f32x4 z = {0.f, 0.f, 0.f, 0.f};
    const f32x4 d = __builtin_amdgcn_mfma_f32_16x16x32_bf16(__builtin_bit_cast(bf16x8, buf[u]), __builtin_bit_cast(bf16x8, qf), z, 0, 0, 0);
    __builtin_amdgcn_sched_barrier(0);
    buf[u] = __builtin_amdgcn_raw_buffer_load_b128(rv, vvo + (u & 3) * 1024, wbase + (((u >> 2) + rot) & 3) * 4096, AUXV);
    __builtin_amdgcn_sched_barrier(0);
    S[u] = (dpp<0x00>(d[0]) + dpp<0x55>(d[1])) + (dpp<0xAA>(d[2]) + dpp<0xFF>(d[3]));
  }
  float m = S[0];
#pragma unroll
  for (int u = 1; u < NU; ++u) m = fmaxf(m, S[u]);
  m = xh_max(m);
  const float mc = m * C2;
  const int jj = (r & 3) + 4 * (r >> 3);
  unsigned msk[4];
#pragma unroll
  for (int c = 0; c < 4; ++c) msk[c] = (c == (jj >> 1)) ? ((jj & 1) ? 0xFFFF0000u : 0x0000FFFFu) : 0u;
  float l = 0.f;
  f32x4 acc = {0.f, 0.f, 0.f, 0.f};
#pragma unroll
  for (int u = 0; u < NU; ++u) {
    const float p = __builtin_amdgcn_exp2f(fmaf(S[u], C2, -mc));
    l += p;
    const unsigned pp = cvtpk(p, p);
    const u32x4 a = {pp & msk[0], pp & msk[1], pp & msk[2], pp & msk[3]};
    acc = __builtin_amdgcn_mfma_f32_16x16x32_bf16(__builtin_bit_cast(bf16x8, a), __builtin_bit_cast(bf16x8, buf[u]), acc, 0, 0, 0);
  }
  l = xh_sum(l);
  *reinterpret_cast<f32x4*>(lds + (wave * 2 + (h & 1)) * REC + 4 + 8 * r + 4 * (h >> 1)) = acc;
  if (lane == 0 || lane == 4) { float* rr = lds + (wave * 2 + (lane >> 2)) * REC; rr[0] = mc; rr[1] = l; }
  if (s == 0 && wave == 0) {
    const unsigned kk = *reinterpret_cast<const unsigned*>(kt + g * HD + 2 * lane);
    const unsigned vv = *reinterpret_cast<const unsigned*>(vt + g * HD + 2 * lane);
    const unsigned q0 = *reinterpret_cast<const unsigned*>(q + (g * 2) * HD + 2 * lane);
    const unsigned q1 = *reinterpret_cast<const unsigned*>(q + (g * 2 + 1) * HD + 2 * lane);
    const float k0 = __uint_as_float(kk << 16), k1 = __uint_as_float(kk & 0xFFFF0000u);
    float x0 = k0 * __uint_as_float(q0 << 16) + k1 * __uint_as_float(q0 & 0xFFFF0000u);
    float x1 = k0 * __uint_as_float(q1 << 16) + k1 * __uint_as_float(q1 & 0xFFFF0000u);
#pragma unroll
    for (int o = 32; o >= 1; o >>= 1) { x0 += __shfl_xor(x0, o); x1 += __shfl_xor(x1, o); }
    const float bias = (1.0f - bf2f(mask[g / HKV])) * -1000000000.0f;
    float* r0 = lds + (WAVES * 2) * REC; float* r1 = r0 + REC;
    r0[4 + 2 * lane] = r1[4 + 2 * lane] = __uint_as_float(vv << 16);
    r0[5 + 2 * lane] = r1[5 + 2 * lane] = __uint_as_float(vv & 0xFFFF0000u);
    if (lane == 0) { r0[0] = (x0 * SCALE + bias) * LOG2E; r0[1] = 1.f; r1[0] = (x1 * SCALE + bias) * LOG2E; r1[1] = 1.f; }
  }
  __syncthreads();
  if (tid < 256) {
    const int hd = tid >> 7, d = tid & 127;
    float M = lds[hd * REC];
#pragma unroll
    for (int w = 1; w < WAVES; ++w) M = fmaxf(M, lds[(w * 2 + hd) * REC]);
    if (s == 0) M = fmaxf(M, lds[(WAVES * 2 + hd) * REC]);
    float L = 0.f, o = 0.f;
#pragma unroll
    for (int w = 0; w < WAVES; ++w) {
      const float* rr = lds + (w * 2 + hd) * REC;
      const float wt = __builtin_amdgcn_exp2f(rr[0] - M);
      L = fmaf(wt, rr[1], L); o = fmaf(wt, rr[4 + d], o);
    }
    gu64* gr = (gu64*)(ws + WS_GRAN) + (size_t)(g * NSPLIT * 2 + hd) * GR;
    if (s != 0) {
      gu64* gp = gr + s * 2 * GR;
      const unsigned long long t = (unsigned long long)tag << 32;
      __hip_atomic_store(gp + d, t | __float_as_uint(o), RLX_AGENT);
      if (d < 2) __hip_atomic_store(gp + 128 + d, t | __float_as_uint(d == 0 ? M : L), RLX_AGENT);
    } else {
      { const float* rr = lds + (WAVES * 2 + hd) * REC;
        const float wt = __builtin_amdgcn_exp2f(rr[0] - M);
        L = fmaf(wt, rr[1], L); o = fmaf(wt, rr[4 + d], o); }
      float po[NSPLIT], pm[NSPLIT], pl[NSPLIT];
      for (;;) {
        bool ok = true;
#pragma unroll
        for (int k = 1; k < NSPLIT; ++k) {
          const unsigned long long x = __hip_atomic_load(gr + k * 2 * GR + d, RLX_AGENT);
          const unsigned long long y = __hip_atomic_load(gr + k * 2 * GR + 128, RLX_AGENT);
          const unsigned long long z = __hip_atomic_load(gr + k * 2 * GR + 129, RLX_AGENT);
          po[k] = __uint_as_float((unsigned)x); pm[k] = __uint_as_float((unsigned)y); pl[k] = __uint_as_float((unsigned)z);
          ok &= ((unsigned)(x >> 32) == tag) & ((unsigned)(y >> 32) == tag) & ((unsigned)(z >> 32) == tag);
        }
        if (__all(ok)) break;
        __builtin_amdgcn_s_sleep(1);
      }
      float M2 = M;
#pragma unroll
      for (int k = 1; k < NSPLIT; ++k) M2 = fmaxf(M2, pm[k]);
      const float w0 = __builtin_amdgcn_exp2f(M - M2);
      L *= w0; o *= w0;
#pragma unroll
      for (int k = 1; k < NSPLIT; ++k) { const float wt = __builtin_amdgcn_exp2f(pm[k] - M2); L = fmaf(wt, pl[k], L); o = fmaf(wt, po[k], o); }
      out[(g * 2 + hd) * HD + d] = (unsigned short)(cvtpk(o / L, 0.f) & 0xFFFFu);
      if (tid == 0) __hip_atomic_store(ep, tag, RLX_AGENT);
    }
  }
}

extern "C" void kernel_launch(void* const* d_in, const int* in_sizes, int n_in, void* d_out, int out_size, void* d_ws, size_t ws_size,
                              hipStream_t stream) {
  tree_attn_decode<<<NGRP * NSPLIT, WAVES * 64, 0, stream>>>((const unsigned short*)d_in[0], (const unsigned short*)d_in[1], (const unsigned short*)d_in[2],
                                                             (const unsigned short*)d_in[3], (const unsigned short*)d_in[4], (const unsigned short*)d_in[5],
                                                             (unsigned short*)d_out, (unsigned char*)d_ws);
}
```

```cpp
#include <hip/hip_runtime.h>
#include <hip/hip_bf16.h>
#include <stdint.h>

#define AUXK 2
#define AUXV 2

typedef __attribute__((ext_vector_type(8))) __bf16 bf16x8;
typedef __attribute__((ext_vector_type(4))) float f32x4;
typedef __attribute__((ext_vector_type(4))) unsigned u32x4;
typedef __attribute__((address_space(1))) unsigned long long gu64;
typedef __attribute__((address_space(1))) unsigned gu32;

constexpr int NB = 2, HQ = 32, HKV = 16, CTX = 8192, HD = 128;
constexpr int NGRP = NB * HKV;
constexpr int NSPLIT = 8, WAVES = 16;
constexpr int TPW = CTX / NSPLIT / WAVES;
constexpr int NU = TPW / 4;
constexpr float SCALE = 0.088388347648318440f, LOG2E = 1.4426950408889634f;
constexpr float C2 = SCALE * LOG2E;
constexpr int REC = 132;
constexpr int GR = 136;
constexpr size_t WS_GRAN = 65536;

__device__ __forceinline__ float bf2f(unsigned short x) { return __uint_as_float((unsigned)x << 16); }
__device__ __forceinline__ unsigned cvtpk(float lo, float hi) {
  typedef __attribute__((ext_vector_type(2))) __bf16 bf16x2;
  typedef __attribute__((ext_vector_type(2))) float f32x2;
  f32x2 v = {lo, hi};
  bf16x2 r = __builtin_convertvector(v, bf16x2);
  return __builtin_bit_cast(unsigned, r);
}
template <int CTRL> __device__ __forceinline__ float dpp(float x) {
  return __builtin_bit_cast(float, __builtin_amdgcn_mov_dpp(__builtin_bit_cast(int, x), CTRL, 0xf, 0xf, true));
}
__device__ __forceinline__ float xh_max(float x) {
  auto s = __builtin_amdgcn_permlane16_swap(__float_as_uint(x), __float_as_uint(x), false, false);
  x = fmaxf(__uint_as_float(s[0]), __uint_as_float(s[1]));
  auto t = __builtin_amdgcn_permlane32_swap(__float_as_uint(x), __float_as_uint(x), false, false);
  return fmaxf(__uint_as_float(t[0]), __uint_as_float(t[1]));
}
__device__ __forceinline__ float xh_sum(float x) {
  auto s = __builtin_amdgcn_permlane16_swap(__float_as_uint(x), __float_as_uint(x), false, false);
  x = __uint_as_float(s[0]) + __uint_as_float(s[1]);
  auto t = __builtin_amdgcn_permlane32_swap(__float_as_uint(x), __float_as_uint(x), false, false);
  return __uint_as_float(t[0]) + __uint_as_float(t[1]);
}
#define RLX_AGENT __ATOMIC_RELAXED, __HIP_MEMORY_SCOPE_AGENT

__global__ void __launch_bounds__(WAVES * 64, 4)
tree_attn_decode(const unsigned short* __restrict__ q, const unsigned short* __restrict__ kc, const unsigned short* __restrict__ vc,
                 const unsigned short* __restrict__ kt, const unsigned short* __restrict__ vt, const unsigned short* __restrict__ mask,
                 unsigned short* __restrict__ out, unsigned char* ws) {
  __shared__ __attribute__((aligned(16))) float lds[(WAVES + 1) * 2 * REC];
  const unsigned long long t_start = __builtin_amdgcn_s_memrealtime();
  __builtin_amdgcn_sched_barrier(0);
  unsigned long long t_ka = 0, t_k0 = 0, t_bar = 0;
  const int bid = blockIdx.x, g = bid >> 3, s = bid & 7;
  const int tid = threadIdx.x, lane = tid & 63, wave = __builtin_amdgcn_readfirstlane(tid >> 6);
  const int r = lane & 15, h = lane >> 4;
  const __amdgpu_buffer_rsrc_t rk = __builtin_amdgcn_make_buffer_rsrc((void*)kc, 0, NGRP * CTX * HD * 2, 0x00020000);
  const __amdgpu_buffer_rsrc_t rv = __builtin_amdgcn_make_buffer_rsrc((void*)vc, 0, NGRP * CTX * HD * 2, 0x00020000);
  const u32x4 qf = *reinterpret_cast<const u32x4*>((const char*)q + (g * 2 + ((r >> 2) & 1)) * 256 + (4 * h + (r & 3)) * 16);
  gu32* ep = (gu32*)(ws + g * 256);
  const unsigned tag = __hip_atomic_load(ep, RLX_AGENT) + 1u;
  __builtin_amdgcn_sched_barrier(0);
  t_ka = __builtin_amdgcn_s_memrealtime();
  __builtin_amdgcn_sched_barrier(0);
  const unsigned wbase = (unsigned)(bid * WAVES + wave) * (TPW * 256);
  const unsigned rot = (unsigned)(bid * WAVES + wave);
  const unsigned kvo = (r >> 2) * 256 + h * 64 + (r & 3) * 16, vvo = lane * 16;
  u32x4 buf[NU];
#pragma unroll
  for (int u = 0; u < NU; ++u)
    buf[u] = __builtin_amdgcn_raw_buffer_load_b128(rk, kvo + (u & 3) * 1024, wbase + (((u >> 2) + rot) & 3) * 4096, AUXK);
  __builtin_amdgcn_sched_barrier(0);
  float S[NU];
#pragma unroll
  for (int u = 0; u < NU; ++u) {
    const f32x4 z = {0.f, 0.f, 0.f, 0.f};
    const f32x4 d = __builtin_amdgcn_mfma_f32_16x16x32_bf16(__builtin_bit_cast(bf16x8, buf[u]), __builtin_bit_cast(bf16x8, qf), z, 0, 0, 0);
    __builtin_amdgcn_sched_barrier(0);
    if (u == 0) { t_k0 = __builtin_amdgcn_s_memrealtime(); __builtin_amdgcn_sched_barrier(0); }
    buf[u] = __builtin_amdgcn_raw_buffer_load_b128(rv, vvo + (u & 3) * 1024, wbase + (((u >> 2) + rot) & 3) * 4096, AUXV);
    __builtin_amdgcn_sched_barrier(0);
    S[u] = (dpp<0x00>(d[0]) + dpp<0x55>(d[1])) + (dpp<0xAA>(d[2]) + dpp<0xFF>(d[3]));
  }
  float m = S[0];
#pragma unroll
  for (int u = 1; u < NU; ++u) m = fmaxf(m, S[u]);
  m = xh_max(m);
  const float mc = m * C2;
  const int jj = (r & 3) + 4 * (r >> 3);
  unsigned msk[4];
#pragma unroll
  for (int c = 0; c < 4; ++c) msk[c] = (c == (jj >> 1)) ? ((jj & 1) ? 0xFFFF0000u : 0x0000FFFFu) : 0u;
  float l = 0.f;
  f32x4 acc = {0.f, 0.f, 0.f, 0.f};
#pragma unroll
  for (int u = 0; u < NU; ++u) {
    const float p = __builtin_amdgcn_exp2f(fmaf(S[u], C2, -mc));
    l += p;
    const unsigned pp = cvtpk(p, p);
    const u32x4 a = {pp & msk[0], pp & msk[1], pp & msk[2], pp & msk[3]};
    acc = __builtin_amdgcn_mfma_f32_16x16x32_bf16(__builtin_bit_cast(bf16x8, a), __builtin_bit_cast(bf16x8, buf[u]), acc, 0, 0, 0);
  }
  l = xh_sum(l);
  *reinterpret_cast<f32x4*>(lds + (wave * 2 + (h & 1)) * REC + 4 + 8 * r + 4 * (h >> 1)) = acc;
  if (lane == 0 || lane == 4) { float* rr = lds + (wave * 2 + (lane >> 2)) * REC; rr[0] = mc; rr[1] = l; }
  if (s == 0 && wave == 0) {
    const unsigned kk = *reinterpret_cast<const unsigned*>(kt + g * HD + 2 * lane);
    const unsigned vv = *reinterpret_cast<const unsigned*>(vt + g * HD + 2 * lane);
    const unsigned q0 = *reinterpret_cast<const unsigned*>(q + (g * 2) * HD + 2 * lane);
    const unsigned q1 = *reinterpret_cast<const unsigned*>(q + (g * 2 + 1) * HD + 2 * lane);
    const float k0 = __uint_as_float(kk << 16), k1 = __uint_as_float(kk & 0xFFFF0000u);
    float x0 = k0 * __uint_as_float(q0 << 16) + k1 * __uint_as_float(q0 & 0xFFFF0000u);
    float x1 = k0 * __uint_as_float(q1 << 16) + k1 * __uint_as_float(q1 & 0xFFFF0000u);
#pragma unroll
    for (int o = 32; o >= 1; o >>= 1) { x0 += __shfl_xor(x0, o); x1 += __shfl_xor(x1, o); }
    const float bias = (1.0f - bf2f(mask[g / HKV])) * -1000000000.0f;
    float* r0 = lds + (WAVES * 2) * REC; float* r1 = r0 + REC;
    r0[4 + 2 * lane] = r1[4 + 2 * lane] = __uint_as_float(vv << 16);
    r0[5 + 2 * lane] = r1[5 + 2 * lane] = __uint_as_float(vv & 0xFFFF0000u);
    if (lane == 0) { r0[0] = (x0 * SCALE + bias) * LOG2E; r0[1] = 1.f; r1[0] = (x1 * SCALE + bias) * LOG2E; r1[1] = 1.f; }
  }
  __syncthreads();
  t_bar = __builtin_amdgcn_s_memrealtime();
  if (tid < 256) {
    const int hd = tid >> 7, d = tid & 127;
    float M = lds[hd * REC];
#pragma unroll
    for (int w = 1; w < WAVES; ++w) M = fmaxf(M, lds[(w * 2 + hd) * REC]);
    if (s == 0) M = fmaxf(M, lds[(WAVES * 2 + hd) * REC]);
    float L = 0.f, o = 0.f;
#pragma unroll
    for (int w = 0; w < WAVES; ++w) {
      const float* rr = lds + (w * 2 + hd) * REC;
      const float wt = __builtin_amdgcn_exp2f(rr[0] - M);
      L = fmaf(wt, rr[1], L); o = fmaf(wt, rr[4 + d], o);
    }
    gu64* gr = (gu64*)(ws + WS_GRAN) + (size_t)(g * NSPLIT * 2 + hd) * GR;
    if (s != 0) {
      gu64* gp = gr + s * 2 * GR;
      const unsigned long long t = (unsigned long long)tag << 32;
      __hip_atomic_store(gp + d, t | __float_as_uint(o), RLX_AGENT);
      if (d < 2) __hip_atomic_store(gp + 128 + d, t | __float_as_uint(d == 0 ? M : L), RLX_AGENT);
    } else {
      { const float* rr = lds + (WAVES * 2 + hd) * REC;
        const float wt = __builtin_amdgcn_exp2f(rr[0] - M);
        L = fmaf(wt, rr[1], L); o = fmaf(wt, rr[4 + d], o); }
      float po[NSPLIT], pm[NSPLIT], pl[NSPLIT];
      for (;;) {
        bool ok = true;
#pragma unroll
        for (int k = 1; k < NSPLIT; ++k) {
          const unsigned long long x = __hip_atomic_load(gr + k * 2 * GR + d, RLX_AGENT);
          const unsigned long long y = __hip_atomic_load(gr + k * 2 * GR + 128, RLX_AGENT);
          const unsigned long long z = __hip_atomic_load(gr + k * 2 * GR + 129, RLX_AGENT);
          po[k] = __uint_as_float((unsigned)x); pm[k] = __uint_as_float((unsigned)y); pl[k] = __uint_as_float((unsigned)z);
          ok &= ((unsigned)(x >> 32) == tag) & ((unsigned)(y >> 32) == tag) & ((unsigned)(z >> 32) == tag);
        }
        if (__all(ok)) break;
        __builtin_amdgcn_s_sleep(1);
      }
      float M2 = M;
#pragma unroll
      for (int k = 1; k < NSPLIT; ++k) M2 = fmaxf(M2, pm[k]);
      const float w0 = __builtin_amdgcn_exp2f(M - M2);
      L *= w0; o *= w0;
#pragma unroll
      for (int k = 1; k < NSPLIT; ++k) { const float wt = __builtin_amdgcn_exp2f(pm[k] - M2); L = fmaf(wt, pl[k], L); o = fmaf(wt, po[k], o); }
      out[(g * 2 + hd) * HD + d] = (unsigned short)(cvtpk(o / L, 0.f) & 0xFFFFu);
      if (tid == 0) __hip_atomic_store(ep, tag, RLX_AGENT);
    }
  }
  if (wave == 0) { const unsigned long long until = t_start + 5000ull; while (__builtin_amdgcn_s_memrealtime() < until) __builtin_amdgcn_s_sleep(1); }
}

extern "C" void kernel_launch(void* const* d_in, const int* in_sizes, int n_in, void* d_out, int out_size, void* d_ws, size_t ws_size,
                              hipStream_t stream) {
  tree_attn_decode<<<NGRP * NSPLIT, WAVES * 64, 0, stream>>>((const unsigned short*)d_in[0], (const unsigned short*)d_in[1], (const unsigned short*)d_in[2],
                                                             (const unsigned short*)d_in[3], (const unsigned short*)d_in[4], (const unsigned short*)d_in[5],
                                                             (unsigned short*)d_out, (unsigned char*)d_ws);
}
```

```cpp
#include <hip/hip_runtime.h>
#include <hip/hip_bf16.h>
#include <stdint.h>

typedef __attribute__((ext_vector_type(8))) __bf16 bf16x8;
typedef __attribute__((ext_vector_type(4))) float f32x4;
typedef __attribute__((ext_vector_type(2))) float f32x2;
typedef __attribute__((ext_vector_type(4))) unsigned u32x4;
typedef __attribute__((address_space(1))) unsigned gu32;
typedef __attribute__((address_space(3))) unsigned lu32;

#define DEPTH 10
#define MAPV 4
#define PSLEEP 3
#define OUTSC1 1
#define STAMP(i)
#define STAMP_DECL
#define STAMP_OUT

constexpr int NB = 2, HQ = 32, HKV = 16, CTX = 8192, HD = 128;
constexpr int NGRP = NB * HKV;
constexpr int NSPLIT = 8, WAVES = 16, LASTW = WAVES - 4;
constexpr int NU = CTX / NSPLIT / WAVES / 4;
constexpr float SCALE = 0.088388347648318440f, LOG2E = 1.4426950408889634f;
constexpr float C2 = SCALE * LOG2E;
constexpr int WS_GRP = 32768, WS_ML = 2048, WS_O = 4096;
constexpr int AUX_NT = 2, AUX_SC1 = 16;
constexpr unsigned OOB = 0x80000000u;

__device__ __forceinline__ unsigned cvtpk(float lo, float hi) {
  typedef __attribute__((ext_vector_type(2))) __bf16 bf16x2;
  f32x2 v = {lo, hi};
  return __builtin_bit_cast(unsigned, __builtin_convertvector(v, bf16x2));
}
template <int CTRL> __device__ __forceinline__ float dpp(float x) {
  return __builtin_bit_cast(float, __builtin_amdgcn_mov_dpp(__builtin_bit_cast(int, x), CTRL, 0xf, 0xf, true));
}
__device__ __forceinline__ float xh_max(float x) {
  auto s = __builtin_amdgcn_permlane16_swap(__float_as_uint(x), __float_as_uint(x), false, false);
  x = fmaxf(__uint_as_float(s[0]), __uint_as_float(s[1]));
  auto t = __builtin_amdgcn_permlane32_swap(__float_as_uint(x), __float_as_uint(x), false, false);
  return fmaxf(__uint_as_float(t[0]), __uint_as_float(t[1]));
}
__device__ __forceinline__ float xh_sum(float x) {
  auto s = __builtin_amdgcn_permlane16_swap(__float_as_uint(x), __float_as_uint(x), false, false);
  x = __uint_as_float(s[0]) + __uint_as_float(s[1]);
  auto t = __builtin_amdgcn_permlane32_swap(__float_as_uint(x), __float_as_uint(x), false, false);
  return __uint_as_float(t[0]) + __uint_as_float(t[1]);
}
__device__ __forceinline__ float max8(float x) { x = fmaxf(x, dpp<0xB1>(x)); x = fmaxf(x, dpp<0x4E>(x)); return fmaxf(x, dpp<0x141>(x)); }
__device__ __forceinline__ float sum8(float x) { x += dpp<0xB1>(x); x += dpp<0x4E>(x); return x + dpp<0x141>(x); }
__device__ __forceinline__ float lo16(unsigned x) { return __uint_as_float(x << 16); }
__device__ __forceinline__ float hi16(unsigned x) { return __uint_as_float(x & 0xFFFF0000u); }
__device__ __forceinline__ float rdlane(float x, int i) { return __int_as_float(__builtin_amdgcn_readlane(__float_as_int(x), i)); }
__device__ __forceinline__ unsigned long long stale(u32x4 v, unsigned tag) {
  return __builtin_amdgcn_uicmp(v[1], tag, 33) | __builtin_amdgcn_uicmp(v[3], tag, 33);
}
#define RLX_AGENT __ATOMIC_RELAXED, __HIP_MEMORY_SCOPE_AGENT
#define RLX_WG __ATOMIC_RELAXED, __HIP_MEMORY_SCOPE_WORKGROUP
#define LDS_DONE() asm volatile("s_waitcnt lgkmcnt(0)" ::: "memory")
#define WG_BARRIER() do { LDS_DONE(); __builtin_amdgcn_s_barrier(); asm volatile("" ::: "memory"); } while (0)

__global__ void __launch_bounds__(WAVES * 64, 4)
tree_attn_decode(const unsigned short* __restrict__ q, const unsigned short* kc, const unsigned short* vc,
                 const unsigned short* __restrict__ kt, const unsigned short* __restrict__ vt, const unsigned short* __restrict__ mask,
                 unsigned short* __restrict__ out, unsigned char* ws) {
  __shared__ __attribute__((aligned(16))) float lds[(WAVES + NSPLIT - 1) * 2 * HD + 4 * WAVES + 4 * NSPLIT + 4];
  float* lds_ml = lds + (WAVES + NSPLIT - 1) * 2 * HD;
  float* lds_rml = lds_ml + 4 * WAVES;
  lu32* cnt = (lu32*)(lds_rml + 4 * NSPLIT);
  STAMP_DECL
  STAMP(0)
  const int bid = blockIdx.x, g = bid >> 3, s = bid & 7;
  const int tid = threadIdx.x, lane = tid & 63, wave = __builtin_amdgcn_readfirstlane(tid >> 6);
  const int r = lane & 15, h = lane >> 4;
  if (tid < 4) cnt[tid] = 0;
  if (wave >= 4) WG_BARRIER();
  const __amdgpu_buffer_rsrc_t rk = __builtin_amdgcn_make_buffer_rsrc((void*)kc, 0, NGRP * CTX * HD * 2, 0x00020000);
  const __amdgpu_buffer_rsrc_t rv = __builtin_amdgcn_make_buffer_rsrc((void*)vc, 0, NGRP * CTX * HD * 2, 0x00020000);
  const unsigned kvo = (r >> 2) * 256 + h * 64 + (r & 3) * 16, vvo = lane * 16;
  const unsigned gbase = (unsigned)bid * (WAVES * NU * 1024);
#define UOFF(u) (gbase + (wave >> 2) * 65536 + ((4 * ((u) >> 2) + ((wave + ((u) >> 2)) & 3) + 4 * (g >> 1)) & 15) * 4096)
#define LOADK(u) __builtin_amdgcn_raw_buffer_load_b128(rk, kvo + ((u) & 3) * 1024, UOFF(u), AUX_NT)
#define LOADV(u) __builtin_amdgcn_raw_buffer_load_b128(rv, vvo + ((u) & 3) * 1024, UOFF(u), AUX_NT)
  const u32x4 qf = *reinterpret_cast<const u32x4*>((const char*)q + (g * 2 + ((r >> 2) & 1)) * 256 + (4 * h + (r & 3)) * 16);
  __builtin_amdgcn_sched_barrier(0);
  u32x4 buf[DEPTH];
#pragma unroll
  for (int u = 0; u < DEPTH; ++u) buf[u] = LOADK(u);
  __builtin_amdgcn_sched_barrier(0);
  if (wave < 4) WG_BARRIER();
  __builtin_amdgcn_sched_barrier(0);
  float S[NU];
#pragma unroll
  for (int u = 0; u < NU; ++u) {
    const f32x4 z = {0.f, 0.f, 0.f, 0.f};
    const f32x4 d = __builtin_amdgcn_mfma_f32_16x16x32_bf16(__builtin_bit_cast(bf16x8, buf[u % DEPTH]), __builtin_bit_cast(bf16x8, qf), z, 0, 0, 0);
    __builtin_amdgcn_sched_barrier(0);
    buf[u % DEPTH] = (u + DEPTH < NU) ? LOADK(u + DEPTH) : LOADV(u + DEPTH - NU);
    __builtin_amdgcn_sched_barrier(0);
    S[u] = (dpp<0x00>(d[0]) + dpp<0x55>(d[1])) + (dpp<0xAA>(d[2]) + dpp<0xFF>(d[3]));
  }
  float m = S[0];
#pragma unroll
  for (int u = 1; u < NU; ++u) m = fmaxf(m, S[u]);
  const float mc = xh_max(m) * C2;
  unsigned pp[NU];
  float l = 0.f;
#pragma unroll
  for (int u = 0; u < NU; ++u) { const float p = __builtin_amdgcn_exp2f(fmaf(S[u], C2, -mc)); l += p; pp[u] = cvtpk(p, p); }
  l = xh_sum(l);
  if ((lane & 59) == 0) *reinterpret_cast<f32x2*>(lds_ml + ((lane >> 2) * WAVES + wave) * 2) = f32x2{mc, l};
  asm volatile("" ::: "memory");
  if (lane == 0) __hip_atomic_fetch_add(cnt, 1u, RLX_WG);
  const int jj = (r & 3) + 4 * (r >> 3);
  unsigned msk[4];
#pragma unroll
  for (int c = 0; c < 4; ++c) msk[c] = (c == (jj >> 1)) ? ((jj & 1) ? 0xFFFF0000u : 0x0000FFFFu) : 0u;
  f32x4 acc = {0.f, 0.f, 0.f, 0.f};
#pragma unroll
  for (int u = 0; u < NU; ++u) {
    const u32x4 a = {pp[u] & msk[0], pp[u] & msk[1], pp[u] & msk[2], pp[u] & msk[3]};
    __builtin_amdgcn_sched_barrier(0);
    acc = __builtin_amdgcn_mfma_f32_16x16x32_bf16(__builtin_bit_cast(bf16x8, a), __builtin_bit_cast(bf16x8, buf[(u + NU) % DEPTH]), acc, 0, 0, 0);
    __builtin_amdgcn_sched_barrier(0);
    if (u + DEPTH < NU) buf[(u + NU) % DEPTH] = LOADV(u + DEPTH);
    __builtin_amdgcn_sched_barrier(0);
  }
  *reinterpret_cast<f32x4*>(lds + wave * 2 * HD + (h & 1) * HD + 8 * r + 4 * (h >> 1)) = acc;
  asm volatile("" ::: "memory");
  if (lane == 0 && wave < LASTW) __hip_atomic_fetch_add(cnt + 1, 1u, RLX_WG);
  LDS_DONE();
  const __amdgpu_buffer_rsrc_t rw = __builtin_amdgcn_make_buffer_rsrc((void*)(ws + g * WS_GRP), 0, WS_GRP, 0x00020000);
  if (wave < 2) {
    const unsigned tag = __hip_atomic_load((gu32*)(ws + g * WS_GRP), RLX_AGENT) + 1u;
    float st = 0.f;
    unsigned vtp = 0;
    if (s == 0) {
      const unsigned kk = *reinterpret_cast<const unsigned*>(kt + g * HD + 2 * lane);
      const unsigned qq = *reinterpret_cast<const unsigned*>(q + (g * 2 + wave) * HD + 2 * lane);
      vtp = *reinterpret_cast<const unsigned*>(vt + g * HD + 2 * lane);
      st = sum8(lo16(kk) * lo16(qq) + hi16(kk) * hi16(qq));
      st += dpp<0x128>(st);
      st = (xh_sum(st) * SCALE + (1.0f - lo16(mask[g / HKV])) * -1000000000.0f) * LOG2E;
    }
    while (__hip_atomic_load(cnt, RLX_WG) != WAVES) __builtin_amdgcn_s_sleep(4);
    asm volatile("" ::: "memory");
    STAMP(1)
    const f32x2 ml = *reinterpret_cast<const f32x2*>(lds_ml + (wave * WAVES + r) * 2);
    float M = max8(ml[0]); M = fmaxf(M, dpp<0x128>(M));
    float wi = __builtin_amdgcn_exp2f(ml[0] - M);
    float L = sum8(wi * ml[1]); L += dpp<0x128>(L);
    if (s != 0) {
      const u32x4 gm = {__float_as_uint(M), tag, __float_as_uint(L), tag};
      __builtin_amdgcn_raw_buffer_store_b128(gm, rw, lane < 8 ? WS_ML + lane * 16 : OOB, (s * 2 + wave) * 128, AUX_SC1);
    }
    while (__hip_atomic_load(cnt + 1, RLX_WG) != LASTW) __builtin_amdgcn_s_sleep(1);
    asm volatile("" ::: "memory");
    const float* lo_ = lds + wave * HD + 2 * lane;
#define REC(i) (*reinterpret_cast<const f32x2*>(lo_ + (i) * 2 * HD))
    f32x2 o = {0.f, 0.f};
#pragma unroll
    for (int w = 0; w < LASTW; ++w) o += rdlane(wi, w) * REC(w);
    STAMP(2)
    if (s != 0) {
      const float w12 = rdlane(wi, LASTW), w13 = rdlane(wi, LASTW + 1), w14 = rdlane(wi, LASTW + 2), w15 = rdlane(wi, LASTW + 3);
      STAMP(3)
      WG_BARRIER();
      STAMP(4)
      o += (w12 * REC(LASTW) + w13 * REC(LASTW + 1)) + (w14 * REC(LASTW + 2) + w15 * REC(LASTW + 3));
      const u32x4 go = {__float_as_uint(o[0]), tag, __float_as_uint(o[1]), tag};
      __builtin_amdgcn_raw_buffer_store_b128(go, rw, WS_O + lane * 16, (s * 2 + wave) * 1024, AUX_SC1);
    } else {
      while (__hip_atomic_load(cnt + 2, RLX_WG) != NSPLIT - 1) __builtin_amdgcn_s_sleep(1);
      asm volatile("" ::: "memory");
      if (tid == 0) __hip_atomic_store((gu32*)(ws + g * WS_GRP), tag, RLX_AGENT);
      const f32x2 rml = *reinterpret_cast<const f32x2*>(lds_rml + (wave * NSPLIT + (lane & 7)) * 2);
      const float mk = (lane & 7) == 0 ? M : rml[0], lk = (lane & 7) == 0 ? L : rml[1];
      const float Mx = max8(fmaxf(mk, st));
      const float wk = __builtin_amdgcn_exp2f(mk - Mx), wt = __builtin_amdgcn_exp2f(st - Mx);
      const float inv = 1.0f / (sum8(wk * lk) + wt);
      const float ck = wk * inv, c0 = rdlane(ck, 0);
      o = o * c0 + f32x2{wt * inv * lo16(vtp), wt * inv * hi16(vtp)};
      float cw[NSPLIT + 3];
#pragma unroll
      for (int i = 0; i < 4; ++i) cw[i] = rdlane(wi * c0, LASTW + i);
#pragma unroll
      for (int k = 1; k < NSPLIT; ++k) cw[3 + k] = rdlane(ck, k);
      unsigned* op = reinterpret_cast<unsigned*>(out + (g * 2 + wave) * HD + 2 * lane);
      STAMP(3)
      WG_BARRIER();
      STAMP(4)
      f32x2 o2 = cw[0] * REC(LASTW);
#pragma unroll
      for (int i = 1; i < NSPLIT + 3; i += 2) { o += cw[i] * REC(LASTW + i); o2 += cw[i + 1] * REC(LASTW + i + 1); }
      o += o2;
      __hip_atomic_store((gu32*)op, cvtpk(o[0], o[1]), RLX_AGENT);
    }
    STAMP(5)
  } else if (wave <= NSPLIT && s == 0) {
    const int k = wave - 1;
    const unsigned tag = __hip_atomic_load((gu32*)(ws + g * WS_GRP), RLX_AGENT) + 1u;
    while (__hip_atomic_load(cnt + 1, RLX_WG) != LASTW) __builtin_amdgcn_s_sleep(8);
    u32x4 a, b;
    for (;;) {
      asm volatile("" ::: "memory");
      a = __builtin_amdgcn_raw_buffer_load_b128(rw, lane < 2 ? WS_ML + lane * 128 : OOB, k * 256, AUX_SC1);
      if ((stale(a, tag) & 3ull) == 0) break;
      __builtin_amdgcn_s_sleep(PSLEEP);
    }
    if (lane < 2) *reinterpret_cast<f32x2*>(lds_rml + (lane * NSPLIT + k) * 2) = f32x2{__uint_as_float(a[0]), __uint_as_float(a[2])};
    asm volatile("" ::: "memory");
    if (lane == 0) __hip_atomic_fetch_add(cnt + 2, 1u, RLX_WG);
    do {
      asm volatile("" ::: "memory");
      a = __builtin_amdgcn_raw_buffer_load_b128(rw, WS_O + lane * 16, k * 2048, AUX_SC1);
      b = __builtin_amdgcn_raw_buffer_load_b128(rw, WS_O + 1024 + lane * 16, k * 2048, AUX_SC1);
    } while (stale(a, tag) | stale(b, tag));
    float* ro = lds + (WAVES - 1 + k) * 2 * HD + 2 * lane;
    *reinterpret_cast<f32x2*>(ro) = f32x2{__uint_as_float(a[0]), __uint_as_float(a[2])};
    *reinterpret_cast<f32x2*>(ro + HD) = f32x2{__uint_as_float(b[0]), __uint_as_float(b[2])};
    LDS_DONE();
  }
  STAMP_OUT
}

extern "C" void kernel_launch(void* const* d_in, const int* in_sizes, int n_in, void* d_out, int out_size, void* d_ws, size_t ws_size,
                              hipStream_t stream) {
  tree_attn_decode<<<NGRP * NSPLIT, WAVES * 64, 0, stream>>>((const unsigned short*)d_in[0], (const unsigned short*)d_in[1], (const unsigned short*)d_in[2],
                                                             (const unsigned short*)d_in[3], (const unsigned short*)d_in[4], (const unsigned short*)d_in[5],
                                                             (unsigned short*)d_out, (unsigned char*)d_ws);
}
```

```cpp
#include <hip/hip_runtime.h>
#include <hip/hip_bf16.h>
#include <stdint.h>

typedef __attribute__((ext_vector_type(8))) __bf16 bf16x8;
typedef __attribute__((ext_vector_type(4))) float f32x4;
typedef __attribute__((ext_vector_type(2))) float f32x2;
typedef __attribute__((ext_vector_type(4))) unsigned u32x4;
typedef __attribute__((address_space(1))) unsigned gu32;
typedef __attribute__((address_space(3))) unsigned lu32;

#define DEPTH 10
#define MAPV 4
#define PSLEEP 16
#define OUTSC1 1
#define KDEPTH 16
#define KPRIO 2
#define CNTU (NU - DEPTH)
#define NLATE 8
#define STAMP(i)
#define STAMP_DECL
#define STAMP_OUT

constexpr int NB = 2, HQ = 32, HKV = 16, CTX = 8192, HD = 128;
constexpr int NGRP = NB * HKV;
constexpr int NSPLIT = 8, WAVES = 16, LASTW = WAVES - NLATE;
constexpr int NU = CTX / NSPLIT / WAVES / 4;
constexpr float SCALE = 0.088388347648318440f, LOG2E = 1.4426950408889634f;
constexpr float C2 = SCALE * LOG2E;
constexpr int WS_GRP = 32768, WS_ML = 2048, WS_O = 4096;
constexpr int AUX_NT = 2, AUX_SC1 = 16;
constexpr unsigned OOB = 0x80000000u;

__device__ __forceinline__ unsigned cvtpk(float lo, float hi) {
  typedef __attribute__((ext_vector_type(2))) __bf16 bf16x2;
  f32x2 v = {lo, hi};
  return __builtin_bit_cast(unsigned, __builtin_convertvector(v, bf16x2));
}
template <int CTRL> __device__ __forceinline__ float dpp(float x) {
  return __builtin_bit_cast(float, __builtin_amdgcn_mov_dpp(__builtin_bit_cast(int, x), CTRL, 0xf, 0xf, true));
}
__device__ __forceinline__ float xh_max(float x) {
  auto s = __builtin_amdgcn_permlane16_swap(__float_as_uint(x), __float_as_uint(x), false, false);
  x = fmaxf(__uint_as_float(s[0]), __uint_as_float(s[1]));
  auto t = __builtin_amdgcn_permlane32_swap(__float_as_uint(x), __float_as_uint(x), false, false);
  return fmaxf(__uint_as_float(t[0]), __uint_as_float(t[1]));
}
__device__ __forceinline__ float xh_sum(float x) {
  auto s = __builtin_amdgcn_permlane16_swap(__float_as_uint(x), __float_as_uint(x), false, false);
  x = __uint_as_float(s[0]) + __uint_as_float(s[1]);
  auto t = __builtin_amdgcn_permlane32_swap(__float_as_uint(x), __float_as_uint(x), false, false);
  return __uint_as_float(t[0]) + __uint_as_float(t[1]);
}
__device__ __forceinline__ float max8(float x) { x = fmaxf(x, dpp<0xB1>(x)); x = fmaxf(x, dpp<0x4E>(x)); return fmaxf(x, dpp<0x141>(x)); }
__device__ __forceinline__ float sum8(float x) { x += dpp<0xB1>(x); x += dpp<0x4E>(x); return x + dpp<0x141>(x); }
__device__ __forceinline__ float lo16(unsigned x) { return __uint_as_float(x << 16); }
__device__ __forceinline__ float hi16(unsigned x) { return __uint_as_float(x & 0xFFFF0000u); }
__device__ __forceinline__ float rdlane(float x, int i) { return __int_as_float(__builtin_amdgcn_readlane(__float_as_int(x), i)); }
__device__ __forceinline__ unsigned long long stale(u32x4 v, unsigned tag) {
  return __builtin_amdgcn_uicmp(v[1], tag, 33) | __builtin_amdgcn_uicmp(v[3], tag, 33);
}
#define RLX_AGENT __ATOMIC_RELAXED, __HIP_MEMORY_SCOPE_AGENT
#define RLX_WG __ATOMIC_RELAXED, __HIP_MEMORY_SCOPE_WORKGROUP
#define LDS_DONE() asm volatile("s_waitcnt lgkmcnt(0)" ::: "memory")
#define WG_BARRIER() do { LDS_DONE(); __builtin_amdgcn_s_barrier(); asm volatile("" ::: "memory"); } while (0)

__global__ void __launch_bounds__(WAVES * 64, 4)
tree_attn_decode(const unsigned short* __restrict__ q, const unsigned short* kc, const unsigned short* vc,
                 const unsigned short* __restrict__ kt, const unsigned short* __restrict__ vt, const unsigned short* __restrict__ mask,
                 unsigned short* __restrict__ out, unsigned char* ws) {
  __shared__ __attribute__((aligned(16))) float lds[(WAVES + NSPLIT - 1) * 2 * HD + 4 * WAVES + 4 * NSPLIT + 4];
  float* lds_ml = lds + (WAVES + NSPLIT - 1) * 2 * HD;
  float* lds_rml = lds_ml + 4 * WAVES;
  lu32* cnt = (lu32*)(lds_rml + 4 * NSPLIT);
  STAMP_DECL
  STAMP(0)
  const int bid = blockIdx.x, g = bid >> 3, s = bid & 7;
  const int tid = threadIdx.x, lane = tid & 63, wave = __builtin_amdgcn_readfirstlane(tid >> 6);
  const int r = lane & 15, h = lane >> 4;
  if (KPRIO) __builtin_amdgcn_s_setprio(KPRIO);
  if (tid < 4) cnt[tid] = 0;
  if (wave >= 4) WG_BARRIER();
  const __amdgpu_buffer_rsrc_t rk = __builtin_amdgcn_make_buffer_rsrc((void*)kc, 0, NGRP * CTX * HD * 2, 0x00020000);
  const __amdgpu_buffer_rsrc_t rv = __builtin_amdgcn_make_buffer_rsrc((void*)vc, 0, NGRP * CTX * HD * 2, 0x00020000);
  const unsigned kvo = (r >> 2) * 256 + h * 64 + (r & 3) * 16, vvo = lane * 16;
  const unsigned gbase = (unsigned)bid * (WAVES * NU * 1024);
#define GENROT 0
#define TROT (g >> 1)
#define UOFF(u) (gbase + (((wave >> 2) + (GENROT)) & 3) * 65536 + ((4 * ((u) >> 2) + ((wave + ((u) >> 2)) & 3) + 4 * (TROT)) & 15) * 4096)
#define LOADK(u) __builtin_amdgcn_raw_buffer_load_b128(rk, kvo + ((u) & 3) * 1024, UOFF(u), AUX_NT)
#define LOADV(u) __builtin_amdgcn_raw_buffer_load_b128(rv, vvo + ((u) & 3) * 1024, UOFF(u), AUX_NT)
  const u32x4 qf = *reinterpret_cast<const u32x4*>((const char*)q + (g * 2 + ((r >> 2) & 1)) * 256 + (4 * h + (r & 3)) * 16);
  __builtin_amdgcn_sched_barrier(0);
  u32x4 kb[KDEPTH];
#pragma unroll
  for (int u = 0; u < KDEPTH; ++u) kb[u] = LOADK(u);
  __builtin_amdgcn_sched_barrier(0);
  if (wave < 4) WG_BARRIER();
  __builtin_amdgcn_sched_barrier(0);
  float S[NU];
#pragma unroll
  for (int u = 0; u < NU; ++u) {
    const f32x4 z = {0.f, 0.f, 0.f, 0.f};
    const f32x4 d = __builtin_amdgcn_mfma_f32_16x16x32_bf16(__builtin_bit_cast(bf16x8, kb[u % KDEPTH]), __builtin_bit_cast(bf16x8, qf), z, 0, 0, 0);
    __builtin_amdgcn_sched_barrier(0);
    if (u + KDEPTH < NU) kb[u % KDEPTH] = LOADK(u + KDEPTH);
    __builtin_amdgcn_sched_barrier(0);
    S[u] = (dpp<0x00>(d[0]) + dpp<0x55>(d[1])) + (dpp<0xAA>(d[2]) + dpp<0xFF>(d[3]));
  }
  float m = S[0];
#pragma unroll
  for (int u = 1; u < NU; ++u) m = fmaxf(m, S[u]);
  const float mc = xh_max(m) * C2;
  unsigned pp[NU];
  float l = 0.f;
#pragma unroll
  for (int u = 0; u < NU; ++u) { const float p = __builtin_amdgcn_exp2f(fmaf(S[u], C2, -mc)); l += p; pp[u] = cvtpk(p, p); }
  l = xh_sum(l);
  if ((lane & 59) == 0) *reinterpret_cast<f32x2*>(lds_ml + ((lane >> 2) * WAVES + wave) * 2) = f32x2{mc, l};
  asm volatile("" ::: "memory");
  if (lane == 0) __hip_atomic_fetch_add(cnt + 3, 1u, RLX_WG);
  const int jj = (r & 3) + 4 * (r >> 3);
  unsigned msk[4];
#pragma unroll
  for (int c = 0; c < 4; ++c) msk[c] = (c == (jj >> 1)) ? ((jj & 1) ? 0xFFFF0000u : 0x0000FFFFu) : 0u;
  __builtin_amdgcn_sched_barrier(0);
  __builtin_amdgcn_s_setprio(0);
  u32x4 buf[DEPTH];
#pragma unroll
  for (int u = 0; u < DEPTH; ++u) buf[u] = LOADV(u);
  __builtin_amdgcn_sched_barrier(0);
  f32x4 acc = {0.f, 0.f, 0.f, 0.f};
#pragma unroll
  for (int u = 0; u < NU; ++u) {
    if (u == CNTU) {
      asm volatile("" ::: "memory");
      if (lane == 0) __hip_atomic_fetch_add(cnt, 1u, RLX_WG);
      __builtin_amdgcn_sched_barrier(0);
    }
    const u32x4 a = {pp[u] & msk[0], pp[u] & msk[1], pp[u] & msk[2], pp[u] & msk[3]};
    __builtin_amdgcn_sched_barrier(0);
    acc = __builtin_amdgcn_mfma_f32_16x16x32_bf16(__builtin_bit_cast(bf16x8, a), __builtin_bit_cast(bf16x8, buf[u % DEPTH]), acc, 0, 0, 0);
    __builtin_amdgcn_sched_barrier(0);
    if (u + DEPTH < NU) buf[u % DEPTH] = LOADV(u + DEPTH);
    __builtin_amdgcn_sched_barrier(0);
  }
  *reinterpret_cast<f32x4*>(lds + wave * 2 * HD + (h & 1) * HD + 8 * r + 4 * (h >> 1)) = acc;
  asm volatile("" ::: "memory");
  if (lane == 0 && wave < LASTW) __hip_atomic_fetch_add(cnt + 1, 1u, RLX_WG);
  LDS_DONE();
  const __amdgpu_buffer_rsrc_t rw = __builtin_amdgcn_make_buffer_rsrc((void*)(ws + g * WS_GRP), 0, WS_GRP, 0x00020000);
  if (wave < 2) {
    const unsigned tag = __hip_atomic_load((gu32*)(ws + g * WS_GRP), RLX_AGENT) + 1u;
    float st = 0.f;
    unsigned vtp = 0;
    if (s == 0) {
      const unsigned kk = *reinterpret_cast<const unsigned*>(kt + g * HD + 2 * lane);
      const unsigned qq = *reinterpret_cast<const unsigned*>(q + (g * 2 + wave) * HD + 2 * lane);
      vtp = *reinterpret_cast<const unsigned*>(vt + g * HD + 2 * lane);
      st = sum8(lo16(kk) * lo16(qq) + hi16(kk) * hi16(qq));
      st += dpp<0x128>(st);
      st = (xh_sum(st) * SCALE + (1.0f - lo16(mask[g / HKV])) * -1000000000.0f) * LOG2E;
    }
    while (__hip_atomic_load(cnt + 3, RLX_WG) != WAVES) __builtin_amdgcn_s_sleep(4);
    asm volatile("" ::: "memory");
    STAMP(1)
    const f32x2 ml = *reinterpret_cast<const f32x2*>(lds_ml + (wave * WAVES + r) * 2);
    float M = max8(ml[0]); M = fmaxf(M, dpp<0x128>(M));
    float wi = __builtin_amdgcn_exp2f(ml[0] - M);
    float L = sum8(wi * ml[1]); L += dpp<0x128>(L);
    if (s != 0) {
      const u32x4 gm = {__float_as_uint(M), tag, __float_as_uint(L), tag};
      __builtin_amdgcn_raw_buffer_store_b128(gm, rw, lane < 8 ? WS_ML + lane * 16 : OOB, (s * 2 + wave) * 128, AUX_SC1);
    }
    while (__hip_atomic_load(cnt + 1, RLX_WG) != LASTW) __builtin_amdgcn_s_sleep(1);
    asm volatile("" ::: "memory");
    const float* lo_ = lds + wave * HD + 2 * lane;
#define REC(i) (*reinterpret_cast<const f32x2*>(lo_ + (i) * 2 * HD))
    f32x2 o = {0.f, 0.f};
#pragma unroll
    for (int w = 0; w < LASTW; ++w) o += rdlane(wi, w) * REC(w);
    STAMP(2)
    if (s != 0) {
      float cw[NLATE];
#pragma unroll
      for (int i = 0; i < NLATE; ++i) cw[i] = rdlane(wi, LASTW + i);
      STAMP(3)
      WG_BARRIER();
      STAMP(4)
      f32x2 o2 = {0.f, 0.f};
#pragma unroll
      for (int i = 0; i < NLATE; i += 2) { o += cw[i] * REC(LASTW + i); o2 += cw[i + 1] * REC(LASTW + i + 1); }
      o += o2;
      const u32x4 go = {__float_as_uint(o[0]), tag, __float_as_uint(o[1]), tag};
      __builtin_amdgcn_raw_buffer_store_b128(go, rw, WS_O + lane * 16, (s * 2 + wave) * 1024, AUX_SC1);
    } else {
      while (__hip_atomic_load(cnt + 2, RLX_WG) != NSPLIT - 1) __builtin_amdgcn_s_sleep(1);
      asm volatile("" ::: "memory");
      if (tid == 0) __hip_atomic_store((gu32*)(ws + g * WS_GRP), tag, RLX_AGENT);
      const f32x2 rml = *reinterpret_cast<const f32x2*>(lds_rml + (wave * NSPLIT + (lane & 7)) * 2);
      const float mk = (lane & 7) == 0 ? M : rml[0], lk = (lane & 7) == 0 ? L : rml[1];
      const float Mx = max8(fmaxf(mk, st));
      const float wk = __builtin_amdgcn_exp2f(mk - Mx), wt = __builtin_amdgcn_exp2f(st - Mx);
      const float inv = 1.0f / (sum8(wk * lk) + wt);
      const float ck = wk * inv, c0 = rdlane(ck, 0);
      o = o * c0 + f32x2{wt * inv * lo16(vtp), wt * inv * hi16(vtp)};
      float cw[NLATE + NSPLIT - 1];
#pragma unroll
      for (int i = 0; i < NLATE; ++i) cw[i] = rdlane(wi * c0, LASTW + i);
#pragma unroll
      for (int k = 1; k < NSPLIT; ++k) cw[NLATE - 1 + k] = rdlane(ck, k);
      unsigned* op = reinterpret_cast<unsigned*>(out + (g * 2 + wave) * HD + 2 * lane);
      STAMP(3)
      WG_BARRIER();
      STAMP(4)
      f32x2 o2 = cw[0] * REC(LASTW);
#pragma unroll
      for (int i = 1; i < NLATE + NSPLIT - 1; i += 2) { o += cw[i] * REC(LASTW + i); o2 += cw[i + 1] * REC(LASTW + i + 1); }
      o += o2;
      __hip_atomic_store((gu32*)op, cvtpk(o[0], o[1]), RLX_AGENT);
    }
    STAMP(5)
  } else if (wave <= NSPLIT && s == 0) {
    const int k = wave - 1;
    const unsigned tag = __hip_atomic_load((gu32*)(ws + g * WS_GRP), RLX_AGENT) + 1u;
    u32x4 a, b;
    for (;;) {
      asm volatile("" ::: "memory");
      a = __builtin_amdgcn_raw_buffer_load_b128(rw, lane < 2 ? WS_ML + lane * 128 : OOB, k * 256, AUX_SC1);
      if ((stale(a, tag) & 3ull) == 0) break;
      __builtin_amdgcn_s_sleep(PSLEEP);
    }
    if (lane < 2) *reinterpret_cast<f32x2*>(lds_rml + (lane * NSPLIT + k) * 2) = f32x2{__uint_as_float(a[0]), __uint_as_float(a[2])};
    asm volatile("" ::: "memory");
    if (lane == 0) __hip_atomic_fetch_add(cnt + 2, 1u, RLX_WG);
    while (__hip_atomic_load(cnt, RLX_WG) != WAVES) __builtin_amdgcn_s_sleep(2);
    do {
      asm volatile("" ::: "memory");
      a = __builtin_amdgcn_raw_buffer_load_b128(rw, WS_O + lane * 16, k * 2048, AUX_SC1);
      b = __builtin_amdgcn_raw_buffer_load_b128(rw, WS_O + 1024 + lane * 16, k * 2048, AUX_SC1);
    } while (stale(a, tag) | stale(b, tag));
    float* ro = lds + (WAVES - 1 + k) * 2 * HD + 2 * lane;
    *reinterpret_cast<f32x2*>(ro) = f32x2{__uint_as_float(a[0]), __uint_as_float(a[2])};
    *reinterpret_cast<f32x2*>(ro + HD) = f32x2{__uint_as_float(b[0]), __uint_as_float(b[2])};
    LDS_DONE();
  }
  STAMP_OUT
}

extern "C" void kernel_launch(void* const* d_in, const int* in_sizes, int n_in, void* d_out, int out_size, void* d_ws, size_t ws_size,
                              hipStream_t stream) {
  tree_attn_decode<<<NGRP * NSPLIT, WAVES * 64, 0, stream>>>((const unsigned short*)d_in[0], (const unsigned short*)d_in[1], (const unsigned short*)d_in[2],
                                                             (const unsigned short*)d_in[3], (const unsigned short*)d_in[4], (const unsigned short*)d_in[5],
                                                             (unsigned short*)d_out, (unsigned char*)d_ws);
}
```

```cpp
#include <hip/hip_runtime.h>
#include <hip/hip_bf16.h>
#include <stdint.h>

typedef __attribute__((ext_vector_type(8))) __bf16 bf16x8;
typedef __attribute__((ext_vector_type(4))) float f32x4;
typedef __attribute__((ext_vector_type(2))) float f32x2;
typedef __attribute__((ext_vector_type(4))) unsigned u32x4;
typedef __attribute__((address_space(1))) unsigned gu32;
typedef __attribute__((address_space(3))) unsigned lu32;

#define DEPTH 8
#define TOTAL 1745
#define TOFS 0
#define GMAP 0
#define NLATE 4
#define PSLEEP 8
#define BARALL 1
#define STAMP(i)
#define STAMP_DECL
#define STAMP_OUT

constexpr int NB = 2, HQ = 32, HKV = 16, CTX = 8192, HD = 128;
constexpr int NGRP = NB * HKV;
constexpr int NSPLIT = 8, WAVES = 16, LASTW = WAVES - NLATE;
constexpr int NU = CTX / NSPLIT / WAVES / 4;
constexpr int NS = NU;
constexpr float SCALE = 0.088388347648318440f, LOG2E = 1.4426950408889634f;
constexpr float C2 = SCALE * LOG2E;
constexpr int WS_GRP = 32768, WS_ML = 2048, WS_O = 4096;
constexpr int AUX_NT = 2, AUX_SC1 = 16;
constexpr unsigned OOB = 0x80000000u;

__device__ __forceinline__ unsigned cvtpk(float lo, float hi) {
  typedef __attribute__((ext_vector_type(2))) __bf16 bf16x2;
  f32x2 v = {lo, hi};
  return __builtin_bit_cast(unsigned, __builtin_convertvector(v, bf16x2));
}
template <int CTRL> __device__ __forceinline__ float dpp(float x) {
  return __builtin_bit_cast(float, __builtin_amdgcn_mov_dpp(__builtin_bit_cast(int, x), CTRL, 0xf, 0xf, true));
}
__device__ __forceinline__ float xh_max(float x) {
  auto s = __builtin_amdgcn_permlane16_swap(__float_as_uint(x), __float_as_uint(x), false, false);
  x = fmaxf(__uint_as_float(s[0]), __uint_as_float(s[1]));
  auto t = __builtin_amdgcn_permlane32_swap(__float_as_uint(x), __float_as_uint(x), false, false);
  return fmaxf(__uint_as_float(t[0]), __uint_as_float(t[1]));
}
__device__ __forceinline__ float xh_sum(float x) {
  auto s = __builtin_amdgcn_permlane16_swap(__float_as_uint(x), __float_as_uint(x), false, false);
  x = __uint_as_float(s[0]) + __uint_as_float(s[1]);
  auto t = __builtin_amdgcn_permlane32_swap(__float_as_uint(x), __float_as_uint(x), false, false);
  return __uint_as_float(t[0]) + __uint_as_float(t[1]);
}
__device__ __forceinline__ float max8(float x) { x = fmaxf(x, dpp<0xB1>(x)); x = fmaxf(x, dpp<0x4E>(x)); return fmaxf(x, dpp<0x141>(x)); }
__device__ __forceinline__ float sum8(float x) { x += dpp<0xB1>(x); x += dpp<0x4E>(x); return x + dpp<0x141>(x); }
__device__ __forceinline__ float lo16(unsigned x) { return __uint_as_float(x << 16); }
__device__ __forceinline__ float hi16(unsigned x) { return __uint_as_float(x & 0xFFFF0000u); }
__device__ __forceinline__ float rdlane(float x, int i) { return __int_as_float(__builtin_amdgcn_readlane(__float_as_int(x), i)); }
__device__ __forceinline__ unsigned long long stale(u32x4 v, unsigned tag) {
  return __builtin_amdgcn_uicmp(v[1], tag, 33) | __builtin_amdgcn_uicmp(v[3], tag, 33);
}
#define RLX_AGENT __ATOMIC_RELAXED, __HIP_MEMORY_SCOPE_AGENT
#define RLX_WG __ATOMIC_RELAXED, __HIP_MEMORY_SCOPE_WORKGROUP
#define CFENCE() asm volatile("" ::: "memory")
#define LDS_DONE() asm volatile("s_waitcnt lgkmcnt(0)" ::: "memory")
#define WG_BARRIER() do { LDS_DONE(); __builtin_amdgcn_s_barrier(); CFENCE(); } while (0)
#define ARRIVE(c) do { CFENCE(); if (lane == 0) __hip_atomic_fetch_add(cnt + (c), 1u, RLX_WG); } while (0)
#define AWAIT(c, n, slp) do { while (__hip_atomic_load(cnt + (c), RLX_WG) != (n)) __builtin_amdgcn_s_sleep(slp); CFENCE(); } while (0)
enum { C_OUT = 0, C_REC = 1, C_REC2 = 2, C_ML = 3, C_TAG = 4 };

__global__ void __launch_bounds__(WAVES * 64, 4)
tree_attn_decode(const unsigned short* __restrict__ q, const unsigned short* kc, const unsigned short* vc,
                 const unsigned short* __restrict__ kt, const unsigned short* __restrict__ vt, const unsigned short* __restrict__ mask,
                 unsigned short* __restrict__ out, unsigned char* ws) {
  __shared__ __attribute__((aligned(16))) float lds[(WAVES + NSPLIT - 1) * 2 * HD + 4 * WAVES + 8];
  float* lds_ml = lds + (WAVES + NSPLIT - 1) * 2 * HD;
  lu32* cnt = (lu32*)(lds_ml + 4 * WAVES);
  STAMP_DECL
  STAMP(0)
  const unsigned tb = (unsigned)__builtin_amdgcn_s_memrealtime();
  const int bid = blockIdx.x;
  const int g = bid >> 3, s = bid & 7;
  const int tid = threadIdx.x, lane = tid & 63, wave = __builtin_amdgcn_readfirstlane(tid >> 6);
  const int r = lane & 15, h = lane >> 4;
  __builtin_amdgcn_s_setprio(2);
  if (tid < 4) cnt[tid] = 0;
  if (wave >= 4) WG_BARRIER();
  const __amdgpu_buffer_rsrc_t rk = __builtin_amdgcn_make_buffer_rsrc((void*)kc, 0, NGRP * CTX * HD * 2, 0x00020000);
  const __amdgpu_buffer_rsrc_t rv = __builtin_amdgcn_make_buffer_rsrc((void*)vc, 0, NGRP * CTX * HD * 2, 0x00020000);
  const __amdgpu_buffer_rsrc_t rw = __builtin_amdgcn_make_buffer_rsrc((void*)(ws + g * WS_GRP), 0, WS_GRP, 0x00020000);
  const unsigned kvo = (r >> 2) * 256 + h * 64 + (r & 3) * 16, vvo = lane * 16;
#define AMAP 10
  const int ge = wave >> 2;
#define TILE(t) (ge * 128 + s * 16 + (((t) + (g >> 1)) & 3) * 4 + ((wave + (t) + (g >> 2)) & 3))
#define LOAD(rs, vo, i) __builtin_amdgcn_raw_buffer_load_b128(rs, (vo) + ((i) & 3) * 1024, (g * 512 + TILE((i) >> 2)) * 4096, AUX_NT)
  const int slot0 = (((wave >> 2) * 64 + (wave & 3)) * 8 + ((bid >> 3) & 7)) * TOTAL + TOFS * 4096;
#define NOW() ((int)((unsigned)__builtin_amdgcn_s_memrealtime() - tb))
#define NAP(n) do { const int t_ = ((slot0 + (n) * 8 * TOTAL) >> 12) - 16; while (NOW() < t_) __builtin_amdgcn_s_sleep(4); } while (0)
#define PACE(n) do { const int t_ = (slot0 + (n) * 8 * TOTAL) >> 12; while (NOW() < t_) {} __builtin_amdgcn_sched_barrier(0); } while (0)
  const u32x4 qf = *reinterpret_cast<const u32x4*>((const char*)q + (g * 2 + ((r >> 2) & 1)) * 256 + (4 * h + (r & 3)) * 16);
  unsigned ep = 0;
  if (wave == WAVES - 1) ep = __hip_atomic_load((gu32*)(ws + g * WS_GRP), RLX_AGENT);
  __builtin_amdgcn_sched_barrier(0);
  u32x4 kb[NS];
  NAP(0);
#pragma unroll
  for (int i = 0; i < NS; ++i) {
    PACE(4 * i);
    if (i >= DEPTH) asm volatile("s_waitcnt vmcnt(%0)" :: "i"(DEPTH - 1) : "memory");
    kb[i] = LOAD(rk, kvo, i);
    __builtin_amdgcn_sched_barrier(0);
    if (i == 2 && wave < 4) WG_BARRIER();
  }
  __builtin_amdgcn_sched_barrier(0);
  float S[NS];
#pragma unroll
  for (int i = 0; i < NS; ++i) {
    const f32x4 z = {0.f, 0.f, 0.f, 0.f};
    const f32x4 d = __builtin_amdgcn_mfma_f32_16x16x32_bf16(__builtin_bit_cast(bf16x8, kb[i]), __builtin_bit_cast(bf16x8, qf), z, 0, 0, 0);
    S[i] = (dpp<0x00>(d[0]) + dpp<0x55>(d[1])) + (dpp<0xAA>(d[2]) + dpp<0xFF>(d[3]));
  }
  float m = S[0];
#pragma unroll
  for (int i = 1; i < NS; ++i) m = fmaxf(m, S[i]);
  const float mc = xh_max(m) * C2;
  unsigned pp[NS];
  float l = 0.f;
#pragma unroll
  for (int i = 0; i < NS; ++i) { const float p = __builtin_amdgcn_exp2f(fmaf(S[i], C2, -mc)); l += p; pp[i] = cvtpk(p, p); }
  l = xh_sum(l);
  if ((lane & 59) == 0) *reinterpret_cast<f32x2*>(lds_ml + ((lane >> 2) * WAVES + wave) * 2) = f32x2{mc, l};
  if (tid == (WAVES - 1) * 64) cnt[C_TAG] = ep + 1u;
  ARRIVE(C_ML);
  __builtin_amdgcn_s_setprio(0);
  if (wave >= WAVES - 2 && s != 0) {
    AWAIT(C_ML, WAVES, 2);
    const f32x2 ml = *reinterpret_cast<const f32x2*>(lds_ml + ((wave & 1) * WAVES + r) * 2);
    const unsigned tag = cnt[C_TAG];
    float M = max8(ml[0]); M = fmaxf(M, dpp<0x128>(M));
    float L = sum8(__builtin_amdgcn_exp2f(ml[0] - M) * ml[1]); L += dpp<0x128>(L);
    const u32x4 gm = {__float_as_uint(M), tag, __float_as_uint(L), tag};
    __builtin_amdgcn_raw_buffer_store_b128(gm, rw, lane < 8 ? WS_ML + lane * 16 : OOB, (s * 2 + (wave & 1)) * 128, AUX_SC1);
  }
  const int jj = (r & 3) + 4 * (r >> 3);
  unsigned msk[4];
#pragma unroll
  for (int c = 0; c < 4; ++c) msk[c] = (c == (jj >> 1)) ? ((jj & 1) ? 0xFFFF0000u : 0x0000FFFFu) : 0u;
  __builtin_amdgcn_sched_barrier(0);
  u32x4 vb[DEPTH];
  NAP(256);
#pragma unroll
  for (int i = 0; i < DEPTH; ++i) { PACE(256 + 4 * i); vb[i] = LOAD(rv, vvo, i); __builtin_amdgcn_sched_barrier(0); }
  f32x4 acc = {0.f, 0.f, 0.f, 0.f};
#pragma unroll
  for (int i = 0; i < NS; ++i) {
    if (i + DEPTH == NS) { ARRIVE(C_OUT); __builtin_amdgcn_sched_barrier(0); }
    const u32x4 a = {pp[i] & msk[0], pp[i] & msk[1], pp[i] & msk[2], pp[i] & msk[3]};
    __builtin_amdgcn_sched_barrier(0);
    acc = __builtin_amdgcn_mfma_f32_16x16x32_bf16(__builtin_bit_cast(bf16x8, a), __builtin_bit_cast(bf16x8, vb[i % DEPTH]), acc, 0, 0, 0);
    __builtin_amdgcn_sched_barrier(0);
    if (i + DEPTH < NS) { PACE(256 + 4 * (i + DEPTH)); vb[i % DEPTH] = LOAD(rv, vvo, i + DEPTH); }
    __builtin_amdgcn_sched_barrier(0);
  }
  *reinterpret_cast<f32x4*>(lds + wave * 2 * HD + (h & 1) * HD + 8 * r + 4 * (h >> 1)) = acc;
  asm volatile("s_cmp_lt_u32 %0, %1\n\ts_cbranch_scc1 1f\n\ts_waitcnt lgkmcnt(0)\n\ts_barrier\n\ts_endpgm\n1:" :: "s"(wave), "i"(LASTW) : "scc", "memory");
  ARRIVE(wave < 8 ? C_REC : C_REC2);
  if (wave < 2) {
    float st = 0.f;
    unsigned vtp = 0;
    if (s == 0) {
      const unsigned kk = *reinterpret_cast<const unsigned*>(kt + g * HD + 2 * lane);
      const unsigned qq = *reinterpret_cast<const unsigned*>(q + (g * 2 + wave) * HD + 2 * lane);
      vtp = *reinterpret_cast<const unsigned*>(vt + g * HD + 2 * lane);
      st = sum8(lo16(kk) * lo16(qq) + hi16(kk) * hi16(qq));
      st += dpp<0x128>(st);
      st = (xh_sum(st) * SCALE + (1.0f - lo16(mask[g / HKV])) * -1000000000.0f) * LOG2E;
    }
    AWAIT(C_ML, WAVES, 2);
    STAMP(1)
    const unsigned tag = cnt[C_TAG];
    const f32x2 ml = *reinterpret_cast<const f32x2*>(lds_ml + (wave * WAVES + r) * 2);
    float M = max8(ml[0]); M = fmaxf(M, dpp<0x128>(M));
    float wi = __builtin_amdgcn_exp2f(ml[0] - M);
    f32x2 o = {0.f, 0.f};
    float ck = 0.f;
    if (s == 0) {
      float L = sum8(wi * ml[1]); L += dpp<0x128>(L);
      u32x4 a;
      for (;;) {
        CFENCE();
        a = __builtin_amdgcn_raw_buffer_load_b128(rw, WS_ML + (lane & 7) * 256, wave * 128, AUX_SC1);
        if ((stale(a, tag) & 0xFEull) == 0) break;
        __builtin_amdgcn_s_sleep(PSLEEP);
      }
      if (tid == 0) __hip_atomic_store((gu32*)(ws + g * WS_GRP), tag, RLX_AGENT);
      const float mk = (lane & 7) == 0 ? M : __uint_as_float(a[0]), lk = (lane & 7) == 0 ? L : __uint_as_float(a[2]);
      const float Mx = max8(fmaxf(mk, st));
      const float wk = __builtin_amdgcn_exp2f(mk - Mx), wt = __builtin_amdgcn_exp2f(st - Mx);
      const float inv = 1.0f / (sum8(wk * lk) + wt);
      ck = wk * inv;
      wi *= rdlane(ck, 0);
      o = f32x2{wt * inv * lo16(vtp), wt * inv * hi16(vtp)};
    }
    STAMP(2)
    AWAIT(C_REC, 8, 4);
    const float* lo_ = lds + wave * HD + 2 * lane;
#define REC(i) (*reinterpret_cast<const f32x2*>(lo_ + (i) * 2 * HD))
#pragma unroll
    for (int w = 0; w < 8; ++w) o += rdlane(wi, w) * REC(w);
    float cw[NLATE + NSPLIT - 1], c8[4];
#pragma unroll
    for (int i = 0; i < NLATE; ++i) { cw[i] = rdlane(wi, LASTW + i); c8[i] = rdlane(wi, 8 + i); }
#pragma unroll
    for (int k = 1; k < NSPLIT; ++k) cw[NLATE - 1 + k] = rdlane(ck, k);
    AWAIT(C_REC2, 4, 1);
    o += (c8[0] * REC(8) + c8[1] * REC(9)) + (c8[2] * REC(10) + c8[3] * REC(11));
    if (s != 0) {
      STAMP(3)
      WG_BARRIER();
      STAMP(4)
      f32x2 o2 = {0.f, 0.f};
#pragma unroll
      for (int i = 0; i < NLATE; i += 2) { o += cw[i] * REC(LASTW + i); o2 += cw[i + 1] * REC(LASTW + i + 1); }
      o += o2;
      const u32x4 go = {__float_as_uint(o[0]), tag, __float_as_uint(o[1]), tag};
      __builtin_amdgcn_raw_buffer_store_b128(go, rw, WS_O + lane * 16, (s * 2 + wave) * 1024, AUX_SC1);
    } else {
      unsigned* op = reinterpret_cast<unsigned*>(out + (g * 2 + wave) * HD + 2 * lane);
      STAMP(3)
      WG_BARRIER();
      STAMP(4)
      f32x2 o2 = cw[0] * REC(LASTW);
#pragma unroll
      for (int i = 1; i < NLATE + NSPLIT - 1; i += 2) { o += cw[i] * REC(LASTW + i); o2 += cw[i + 1] * REC(LASTW + i + 1); }
      o += o2;
      __hip_atomic_store((gu32*)op, cvtpk(o[0], o[1]), RLX_AGENT);
    }
    STAMP(5)
  } else if (wave <= NSPLIT && s == 0) {
    const int k = wave - 1;
    AWAIT(C_OUT, WAVES, 2);
    const unsigned tag = cnt[C_TAG];
    u32x4 a, b;
    do {
      CFENCE();
      a = __builtin_amdgcn_raw_buffer_load_b128(rw, WS_O + lane * 16, k * 2048, AUX_SC1);
      b = __builtin_amdgcn_raw_buffer_load_b128(rw, WS_O + 1024 + lane * 16, k * 2048, AUX_SC1);
    } while (stale(a, tag) | stale(b, tag));
    float* ro = lds + (WAVES - 1 + k) * 2 * HD + 2 * lane;
    *reinterpret_cast<f32x2*>(ro) = f32x2{__uint_as_float(a[0]), __uint_as_float(a[2])};
    *reinterpret_cast<f32x2*>(ro + HD) = f32x2{__uint_as_float(b[0]), __uint_as_float(b[2])};
    LDS_DONE();
    if (BARALL) __builtin_amdgcn_s_barrier();
    __builtin_amdgcn_endpgm();
  }
  STAMP_OUT
}

extern "C" void kernel_launch(void* const* d_in, const int* in_sizes, int n_in, void* d_out, int out_size, void* d_ws, size_t ws_size,
                              hipStream_t stream) {
  tree_attn_decode<<<NGRP * NSPLIT, WAVES * 64, 0, stream>>>((const unsigned short*)d_in[0], (const unsigned short*)d_in[1], (const unsigned short*)d_in[2],
                                                             (const unsigned short*)d_in[3], (const unsigned short*)d_in[4], (const unsigned short*)d_in[5],
                                                             (unsigned short*)d_out, (unsigned char*)d_ws);
}
```

```cpp
#include <hip/hip_runtime.h>
#include <hip/hip_bf16.h>
#include <stdint.h>

typedef __attribute__((ext_vector_type(8))) __bf16 bf16x8;
typedef __attribute__((ext_vector_type(4))) float f32x4;
typedef __attribute__((ext_vector_type(2))) float f32x2;
typedef __attribute__((ext_vector_type(4))) unsigned u32x4;
typedef __attribute__((ext_vector_type(2))) unsigned u32x2;
typedef __attribute__((ext_vector_type(8))) unsigned long long u64x8;
#define GAS __attribute__((address_space(1)))
typedef GAS unsigned char gu8;
typedef GAS unsigned short gu16;
typedef GAS unsigned gu32;
typedef GAS unsigned long long gu64;
typedef __attribute__((address_space(3))) float lf32;

#define TOTAL 1706
#define HEAD 25
#define PDELAY 90
#define POWN 15
#define LAGL 40
#define LAG2 40
#define GR2 40
#define NSKIP 2
#define NFREE 4
#define DPROBE 1
#define INJC 0
#define INJR 0
#define INJS 0
#define PATC 0x40, 0x66f, 0x6d4, 0x4e5, 0x200, 0xe1, 0x2a5, 0x28, 0x2948, 0x1029, 0x33cd, 0x829, 0xc9d, 0x46c, 0x805, 0x78f
#define STAMP(i)
#define STAMP_DECL
#define STAMP_OUT

constexpr int NB = 2, HQ = 32, HKV = 16, CTX = 8192, HD = 128;
constexpr int NGRP = NB * HKV;
constexpr int NSPLIT = 8, WAVES = 5;
constexpr float SCALE = 0.088388347648318440f, LOG2E = 1.4426950408889634f;
constexpr float C2 = SCALE * LOG2E;
constexpr int WS_O = 0, WS_ML = 262144, WS_EP = WS_ML + 65536, WS_END = WS_EP + NGRP * 4352;
constexpr int AUX_NT = 2, AUX_SC1 = 16;
constexpr unsigned PCOL[16] = {PATC};
constexpr unsigned OOB = 0x80000000u, EMPTY = 0xFFFFFFFFu;

__device__ __forceinline__ unsigned cvtpk(float lo, float hi) {
  typedef __attribute__((ext_vector_type(2))) __bf16 bf16x2;
  f32x2 v = {lo, hi};
  return __builtin_bit_cast(unsigned, __builtin_convertvector(v, bf16x2));
}
template <int CTRL> __device__ __forceinline__ float dpp(float x) {
  return __builtin_bit_cast(float, __builtin_amdgcn_mov_dpp(__builtin_bit_cast(int, x), CTRL, 0xf, 0xf, true));
}
template <int I> __device__ __forceinline__ float rowb(float x) { return dpp<0x150 + I>(x); }
__device__ __forceinline__ float x16_sum(float x) {
  auto s = __builtin_amdgcn_permlane16_swap(__float_as_uint(x), __float_as_uint(x), false, false);
  return __uint_as_float(s[0]) + __uint_as_float(s[1]);
}
__device__ __forceinline__ float xh_max(float x) {
  auto s = __builtin_amdgcn_permlane16_swap(__float_as_uint(x), __float_as_uint(x), false, false);
  x = fmaxf(__uint_as_float(s[0]), __uint_as_float(s[1]));
  auto t = __builtin_amdgcn_permlane32_swap(__float_as_uint(x), __float_as_uint(x), false, false);
  return fmaxf(__uint_as_float(t[0]), __uint_as_float(t[1]));
}
__device__ __forceinline__ float max8(float x) { x = fmaxf(x, dpp<0xB1>(x)); x = fmaxf(x, dpp<0x4E>(x)); return fmaxf(x, dpp<0x141>(x)); }
__device__ __forceinline__ float sum8(float x) { x += dpp<0xB1>(x); x += dpp<0x4E>(x); return x + dpp<0x141>(x); }
__device__ __forceinline__ float sum16(float x) { x = sum8(x); return x + dpp<0x128>(x); }
__device__ __forceinline__ float lo16(unsigned x) { return __uint_as_float(x << 16); }
__device__ __forceinline__ float hi16(unsigned x) { return __uint_as_float(x & 0xFFFF0000u); }
__device__ __forceinline__ unsigned umax4(u32x4 v) { return max(max(v[0], v[1]), max(v[2], v[3])); }
#define RLX_AGENT __ATOMIC_RELAXED, __HIP_MEMORY_SCOPE_AGENT
#define RLX_WG __ATOMIC_RELAXED, __HIP_MEMORY_SCOPE_WORKGROUP
#define CFENCE() asm volatile("" ::: "memory")
#define SB() __builtin_amdgcn_sched_barrier(0)
#define WG_BARRIER() asm volatile("s_waitcnt lgkmcnt(0)\n\ts_barrier" ::: "memory")
#define PACE_TXT "1:\n\ts_memrealtime vcc\n\ts_waitcnt lgkmcnt(0)\n\ts_lshl_b32 vcc_lo, vcc_lo, 6\n\t" \
    "s_sub_i32 vcc_lo, vcc_lo, %0\n\ts_cmp_lt_i32 vcc_lo, 0\n\ts_cbranch_scc1 1b\n\ts_add_i32 %0, %0, %1"
#define PACE(t, d) asm volatile(PACE_TXT : "+s"(t) : "s"(d) : "vcc", "scc", "memory")
#define PACEC(t, d, thr, thr2) asm volatile(PACE_TXT "\n\ts_cmpk_ge_i32 vcc_lo, %3\n\ts_cbranch_scc1 3f\n\ts_cmpk_ge_i32 vcc_lo, %2\n\ts_cbranch_scc1 2f\n\t" \
    "s_waitcnt vmcnt(4)\n2:\n\ts_waitcnt vmcnt(5)\n3:\n\ts_waitcnt vmcnt(6)" : "+s"(t) : "s"(d), "i"(thr), "i"(thr2) : "vcc", "scc", "memory")
#define PACEE(t, d, thr, thr2) asm volatile(PACE_TXT "\n\ts_cmp_ge_i32 vcc_lo, %3\n\ts_cbranch_scc1 3f\n\ts_cmpk_ge_i32 vcc_lo, %2\n\ts_cbranch_scc1 2f\n\t" \
    "s_waitcnt vmcnt(4)\n2:\n\ts_waitcnt vmcnt(5)\n3:\n\ts_waitcnt vmcnt(6)" : "+s"(t) : "s"(d), "i"(thr), "s"(thr2) : "vcc", "scc", "memory")
#define ORD(i) (((i) & 3) == 0 ? 1 : ((i) & 3) == 1 ? 3 : ((i) & 3) == 2 ? 2 : 4)
#define THR(i) (LAGL * 16 * ORD(i))
#define THR2(i) (LAG2 * 64 + GR2 * 16 * ORD(i))
#define NOW64() ((unsigned)__builtin_amdgcn_s_memrealtime() << 6)
#define NAP(t) do { const unsigned t_ = (t); while ((int)(NOW64() - t_) < 0) __builtin_amdgcn_s_sleep(4); } while (0)

__global__ void __launch_bounds__(WAVES * 64)
tree_attn_decode(const unsigned short*, const unsigned short*, const unsigned short*, const unsigned short*, const unsigned short*,
                 const unsigned short*, unsigned short*, unsigned char*) {
  u64x8 args;
  unsigned long long tb64;
  asm volatile("s_load_dwordx16 %0, %2, 0x0\n\ts_memrealtime %1" : "=&s"(args), "=&s"(tb64) : "s"(__builtin_amdgcn_kernarg_segment_ptr()));
  SB();
  __shared__ __attribute__((aligned(16))) float lds[2048 + 1024 + 8];
  float* lds_o = lds + 2048;
  float* lds_m = lds_o + 1024;
  STAMP_DECL
  STAMP(0)
  const int bid = blockIdx.x, g = bid >> 3, s = bid & 7;
  const int tid = threadIdx.x, lane = tid & 63, wave = __builtin_amdgcn_readfirstlane(tid >> 6);
  const int r = lane & 15, h = lane >> 4;
  const unsigned kvo = (r >> 2) * 256 + h * 64 + (r & 3) * 16, vvo = lane * 16;
  unsigned c0 = 0;
#pragma unroll
  for (int b = 0; b < 2; ++b) c0 ^= ((wave >> b) & 1) ? PCOL[6 + b] : 0u;
#pragma unroll
  for (int b = 0; b < 8; ++b) c0 ^= ((bid >> b) & 1) ? PCOL[8 + b] : 0u;
  const unsigned reg = ((c0 & 0x7FF) << 10) | (g << 21);
#define LOFF(i) (((((i) & 1) ? PCOL[0] : 0u) ^ (((i) & 2) ? PCOL[1] : 0u) ^ (((i) & 4) ? PCOL[2] : 0u)) << 10)
#define BOFF(b) (reg ^ (((((b) & 1) ? PCOL[3] : 0u) ^ (((b) & 2) ? PCOL[4] : 0u) ^ (((b) & 4) ? PCOL[5] : 0u)) << 10))
  const unsigned t0 = ((wave * 8 + (c0 >> 11)) * TOTAL >> 6) + HEAD * 64, dt = TOTAL / 2;
  const unsigned qo = (g * 2 + ((r >> 2) & 1)) * 256 + (4 * h + (r & 3)) * 16;
  asm volatile("" :: "s"(t0), "s"(reg), "v"(kvo), "v"(qo));
  SB();
  asm volatile("s_waitcnt lgkmcnt(0)" : "+s"(args), "+s"(tb64));
  const gu16* q = (const gu16*)args[0];
  gu8* ws = (gu8*)args[7];
  unsigned tb = (unsigned)tb64 << 6;
  if (INJS) tb += ((((s + 8 * (tb >> 18)) * 2654435761u) >> 16) % (unsigned)(INJS + 1)) * 64;
  if (wave >= 4) {
    const __amdgpu_buffer_rsrc_t rw = __builtin_amdgcn_make_buffer_rsrc((void*)ws, 0, WS_END, 0x00020000);
    lds_m[lane & 7] = __uint_as_float(EMPTY);
    WG_BARRIER();
    NAP(tb + 250 * 64);
    const unsigned tag = __hip_atomic_load((gu32*)(ws + WS_EP + g * 4352), RLX_AGENT) + 1u;
    __builtin_amdgcn_raw_buffer_store_b128(u32x4{EMPTY, EMPTY, EMPTY, EMPTY}, rw, lane * 16, WS_O + bid * 1024, AUX_SC1);
    float st = 0.f;
    u32x2 vv = {0u, 0u};
    if (s == 0) {
      const u32x2 kk = *(const GAS u32x2*)((const gu16*)args[3] + g * HD + 4 * (lane & 31));
      const u32x2 qq = *(const GAS u32x2*)(q + g * 2 * HD + 4 * lane);
      vv = *(const GAS u32x2*)((const gu16*)args[4] + g * HD + 4 * (lane & 31));
      const float bias = (1.0f - lo16(((const gu16*)args[5])[g / HKV])) * -1000000000.0f;
      st = (lo16(kk[0]) * lo16(qq[0]) + hi16(kk[0]) * hi16(qq[0])) + (lo16(kk[1]) * lo16(qq[1]) + hi16(kk[1]) * hi16(qq[1]));
      st = (x16_sum(sum16(st)) * SCALE + bias) * LOG2E;
    }
    asm volatile("s_waitcnt vmcnt(0)" ::: "memory");
    f32x4 w4;
    do {
      __builtin_amdgcn_s_sleep(8);
      CFENCE();
      w4 = *reinterpret_cast<const f32x4*>(lds_m + (lane >> 5) * 4);
    } while (__builtin_amdgcn_uicmp(umax4(__builtin_bit_cast(u32x4, w4)), EMPTY, 32));
    CFENCE();
    STAMP(1)
    const float M = fmaxf(fmaxf(w4[0], w4[1]), fmaxf(w4[2], w4[3]));
#pragma unroll
    for (int c = 0; c < 4; ++c) w4[c] = __builtin_amdgcn_exp2f(w4[c] - M);
    float L = 0.f;
#pragma unroll 1
    for (int k = 0; k < 8; ++k) {
      const f32x4 x = *reinterpret_cast<const f32x4*>(lds + (lane >> 5) * 1024 + k * 128 + (lane & 31) * 4);
      L += (__builtin_amdgcn_exp2f(fmaf(x[0], C2, -M)) + __builtin_amdgcn_exp2f(fmaf(x[1], C2, -M))) +
           (__builtin_amdgcn_exp2f(fmaf(x[2], C2, -M)) + __builtin_amdgcn_exp2f(fmaf(x[3], C2, -M)));
    }
    L = x16_sum(sum16(L));
#define REC(i) (*reinterpret_cast<const f32x4*>(lds_o + (i) * 2 * HD + 4 * lane))
    if (s != 0) {
      const u32x4 gm = {(lane & 1) ? tb : __float_as_uint(M), tag, __float_as_uint(L), tag};
      __builtin_amdgcn_raw_buffer_store_b128(gm, rw, (lane & 24) == 0 ? (lane >> 5) * 128 + (lane & 7) * 16 : OOB, WS_ML + bid * 256, AUX_SC1);
      STAMP(3)
      WG_BARRIER();
      STAMP(4)
      u32x4 go = __builtin_bit_cast(u32x4, (w4[0] * REC(0) + w4[1] * REC(1)) + (w4[2] * REC(2) + w4[3] * REC(3)));
#pragma unroll
      for (int c = 0; c < 4; ++c) go[c] = min(go[c], EMPTY - 1u);
      __builtin_amdgcn_raw_buffer_store_b128(go, rw, lane * 16, WS_O + bid * 1024, AUX_SC1);
    } else {
      NAP(NOW64() + 150 * 64);
      u32x4 a;
      for (;;) {
        CFENCE();
        a = __builtin_amdgcn_raw_buffer_load_b128(rw, (lane & 7) * 256 + (lane >> 5) * 128 + (lane & 8) * 2, WS_ML + bid * 256, AUX_SC1);
        if (((__builtin_amdgcn_uicmp(a[1], tag, 33) | __builtin_amdgcn_uicmp(a[3], tag, 33)) & 0xFEFEFEFEFEFEFEFEull) == 0) break;
        __builtin_amdgcn_s_sleep(16);
      }
      if (lane == 0) __hip_atomic_store((gu32*)(ws + WS_EP + g * 4352), tag, RLX_AGENT);
      const float mk = (lane & 7) == 0 ? M : __uint_as_float(a[0]), lk = (lane & 7) == 0 ? L : __uint_as_float(a[2]);
      const float Mx = max8(fmaxf(mk, st));
      const float wk = __builtin_amdgcn_exp2f(mk - Mx), wt = __builtin_amdgcn_exp2f(st - Mx);
      const float inv = 1.0f / (sum8(wk * lk) + wt), ck = wk * inv;
      w4 *= rowb<0>(ck);
      const float c1 = rowb<1>(ck), c2 = rowb<2>(ck), c3 = rowb<3>(ck), c4 = rowb<4>(ck), c5 = rowb<5>(ck), c6 = rowb<6>(ck), c7 = rowb<7>(ck);
      f32x4 o = rowb<0>(wt * inv) * f32x4{lo16(vv[0]), hi16(vv[0]), lo16(vv[1]), hi16(vv[1])};
      const float skew = max8((lane & 7) == 0 ? 0.f : (float)(int)(a[0] - tb));
      const unsigned te = tb + (HEAD + TOTAL + PDELAY) * 64 + __builtin_amdgcn_readlane((int)skew, 8);
      gu64* op = (gu64*)((gu16*)args[6] + g * 2 * HD + 4 * lane);
      STAMP(3)
      WG_BARRIER();
      STAMP(4)
      unsigned tp = NOW64() + POWN * 64;
      o += (w4[0] * REC(0) + w4[1] * REC(1)) + (w4[2] * REC(2) + w4[3] * REC(3));
      if ((int)(te - tp) > 0) tp = te;
      PACE(tp, dt);
      u32x4 po[NSPLIT];
      unsigned mx;
      do {
        CFENCE();
#pragma unroll
        for (int k = 1; k < NSPLIT; ++k) po[k] = __builtin_amdgcn_raw_buffer_load_b128(rw, lane * 16 + (k & 3) * 1024, WS_O + bid * 1024 + (k >> 2) * 4096, AUX_SC1);
        mx = umax4(po[1]);
#pragma unroll
        for (int k = 2; k < NSPLIT; ++k) mx = max(mx, umax4(po[k]));
      } while (__builtin_amdgcn_uicmp(mx, EMPTY, 32));
#define PO(k) __builtin_bit_cast(f32x4, po[k])
      o = ((o + c1 * PO(1)) + (c2 * PO(2) + c3 * PO(3))) + ((c4 * PO(4) + c5 * PO(5)) + (c6 * PO(6) + c7 * PO(7)));
      __hip_atomic_store(op, (unsigned long long)cvtpk(o[0], o[1]) | ((unsigned long long)cvtpk(o[2], o[3]) << 32), RLX_AGENT);
      if (DPROBE) { asm volatile("s_waitcnt vmcnt(0)" ::: "memory"); const unsigned until = tb + 3000 * 64 + 5 * (NOW64() - tb - 1800 * 64); while ((int)(NOW64() - until) < 0) __builtin_amdgcn_s_sleep(1); }
    }
    STAMP(5)
  } else {
    if (INJC | INJR | INJS) { const unsigned dl = 25 + INJC + (INJR ? (((bid + (tb >> 18)) * 2654435761u) >> 16) % (unsigned)(INJR + 1) : 0u); while ((int)(NOW64() - tb) < (int)dl * 64) {} }
    const __amdgpu_buffer_rsrc_t rk = __builtin_amdgcn_make_buffer_rsrc((void*)args[1], 0, NGRP * CTX * HD * 2, 0x00020000);
    const __amdgpu_buffer_rsrc_t rv = __builtin_amdgcn_make_buffer_rsrc((void*)args[2], 0, NGRP * CTX * HD * 2, 0x00020000);
    unsigned tn = tb + t0 + NSKIP * dt;
    u32x4 buf[8], qf;
#pragma unroll
    for (int i = 0; i < 8; ++i) {
      if (i >= 5) PACEC(tn, dt, THR(i), THR2(i)); else if (i >= NSKIP) PACE(tn, dt);
      buf[i] = __builtin_amdgcn_raw_buffer_load_b128(rk, kvo, reg ^ LOFF(i), AUX_NT);
      SB();
      if (i == 0) { qf = *(const GAS u32x4*)((const gu8*)q + qo); SB(); }
      if (i == 2) __builtin_amdgcn_s_barrier();
    }
    const int jj = (r & 3) + 4 * (r >> 3);
    unsigned msk[4];
#pragma unroll
    for (int c = 0; c < 4; ++c) msk[c] = (c == (jj >> 1)) ? ((jj & 1) ? 0xFFFF0000u : 0x0000FFFFu) : 0u;
    lf32* mp = (lf32*)(lds_m + (lane & 4) + wave);
    asm volatile("" : "+v"(msk[0]), "+v"(msk[1]), "+v"(msk[2]), "+v"(msk[3]), "+v"(mp));
    float* sp = lds + (((lane >> 2) & 1) * 4 + wave) * 256 + h * 8;
    float m = -INFINITY;
#pragma unroll 1
    for (int b = 1; b <= 8; ++b) {
      const __amdgpu_buffer_rsrc_t rs = __builtin_amdgcn_make_buffer_rsrc((void*)(b < 8 ? args[1] : args[2]), 0, NGRP * CTX * HD * 2, 0x00020000);
      const unsigned vo = b < 8 ? kvo : vvo, bo = BOFF(b);
      float S[8];
#pragma unroll
      for (int i = 0; i < 8; ++i) {
        const f32x4 z = {0.f, 0.f, 0.f, 0.f};
        const f32x4 d = __builtin_amdgcn_mfma_f32_16x16x32_bf16(__builtin_bit_cast(bf16x8, buf[i]), __builtin_bit_cast(bf16x8, qf), z, 0, 0, 0);
        S[i] = (dpp<0x00>(d[0]) + dpp<0x55>(d[1])) + (dpp<0xAA>(d[2]) + dpp<0xFF>(d[3]));
        m = fmaxf(m, S[i]);
        asm volatile("" : "+v"(m));
        if ((i & 3) == 3) *reinterpret_cast<f32x4*>(sp + (b - 1) * 32 + (i & 4)) = f32x4{S[i - 3], S[i - 2], S[i - 1], S[i]};
        SB();
        PACEC(tn, dt, THR(i), THR2(i));
        buf[i] = __builtin_amdgcn_raw_buffer_load_b128(rs, vo, bo ^ LOFF(i), AUX_NT);
        SB();
      }
    }
    f32x4 s0 = *reinterpret_cast<const f32x4*>(sp), s1 = *reinterpret_cast<const f32x4*>(sp + 4);
    const float mc = xh_max(m) * C2;
    CFENCE();
    *mp = __uint_as_float(min(__float_as_uint(mc), EMPTY - 1u));
    f32x4 acc = {0.f, 0.f, 0.f, 0.f};
#define PV(sc, i) do { const float p_ = __builtin_amdgcn_exp2f(fmaf(sc, C2, -mc)); const unsigned pp_ = cvtpk(p_, p_); \
      const u32x4 a_ = {pp_ & msk[0], pp_ & msk[1], pp_ & msk[2], pp_ & msk[3]}; \
      acc = __builtin_amdgcn_mfma_f32_16x16x32_bf16(__builtin_bit_cast(bf16x8, a_), __builtin_bit_cast(bf16x8, buf[i]), acc, 0, 0, 0); SB(); } while (0)
#pragma unroll 1
    for (int b = 1; b < 8; ++b) {
      const unsigned bo = BOFF(b);
      const int te = b == 7 ? 0 : (LAG2 + GR2) * 64;
#pragma unroll
      for (int i = 0; i < 8; ++i) {
        PV(i < 4 ? s0[i & 3] : s1[i & 3], i);
        if (i == 3) s0 = *reinterpret_cast<const f32x4*>(sp + b * 32);
        if (i == 7) s1 = *reinterpret_cast<const f32x4*>(sp + b * 32 + 4);
        if (i >= 8 - NFREE) PACEE(tn, dt, THR(i), te); else PACEC(tn, dt, THR(i), THR2(i));
        buf[i] = __builtin_amdgcn_raw_buffer_load_b128(rv, vvo, bo ^ LOFF(i), AUX_NT);
        SB();
      }
    }
#pragma unroll
    for (int i = 0; i < 8; ++i) PV(i < 4 ? s0[i & 3] : s1[i & 3], i);
    *reinterpret_cast<f32x4*>(lds_o + wave * 2 * HD + (h & 1) * HD + 8 * r + 4 * (h >> 1)) = acc;
    WG_BARRIER();
  }
  STAMP_OUT
}

extern "C" void kernel_launch(void* const* d_in, const int* in_sizes, int n_in, void* d_out, int out_size, void* d_ws, size_t ws_size,
                              hipStream_t stream) {
  tree_attn_decode<<<NGRP * NSPLIT, WAVES * 64, 0, stream>>>((const unsigned short*)d_in[0], (const unsigned short*)d_in[1], (const unsigned short*)d_in[2],
                                                             (const unsigned short*)d_in[3], (const unsigned short*)d_in[4], (const unsigned short*)d_in[5],
                                                             (unsigned short*)d_out, (unsigned char*)d_ws);
}
```

```cpp
#include <hip/hip_runtime.h>
#include <hip/hip_bf16.h>
#include <stdint.h>

typedef __attribute__((ext_vector_type(8))) __bf16 bf16x8;
typedef __attribute__((ext_vector_type(4))) float f32x4;
typedef __attribute__((ext_vector_type(2))) float f32x2;
typedef __attribute__((ext_vector_type(4))) unsigned u32x4;
typedef __attribute__((ext_vector_type(2))) unsigned u32x2;
typedef __attribute__((ext_vector_type(8))) unsigned long long u64x8;
#define GAS __attribute__((address_space(1)))
typedef GAS unsigned char gu8;
typedef GAS unsigned short gu16;
typedef GAS unsigned gu32;
typedef GAS unsigned long long gu64;
typedef __attribute__((address_space(3))) float lf32;

#define TOTAL 1716
#define HEAD 5
#define PDELAY 90
#define POWN 15
#define LAGL 40
#define LAG2 40
#define GR2 40
#define NSKIP 2
#define NFREE 4
#define DPROBE 0
#define INJC 0
#define INJR 0
#define INJS 0
#define PATC 0x40, 0x66f, 0x6d4, 0x4e5, 0x200, 0xe1, 0x2a5, 0x28, 0x2948, 0x1029, 0x33cd, 0x829, 0xc9d, 0x46c, 0x805, 0x78f
#define CODEPF 1
#define STAMP(i)
#define STAMP_DECL
#define STAMP_OUT

constexpr int NB = 2, HQ = 32, HKV = 16, CTX = 8192, HD = 128;
constexpr int NGRP = NB * HKV;
constexpr int NSPLIT = 8, WAVES = 5;
constexpr float SCALE = 0.088388347648318440f, LOG2E = 1.4426950408889634f;
constexpr float C2 = SCALE * LOG2E;
constexpr int WS_O = 0, WS_ML = 262144, WS_EP = WS_ML + 65536, WS_END = WS_EP + NGRP * 4352;
constexpr int AUX_NT = 2, AUX_SC1 = 16;
constexpr unsigned PCOL[16] = {PATC};
constexpr unsigned OOB = 0x80000000u, EMPTY = 0xFFFFFFFFu;

__device__ __forceinline__ unsigned cvtpk(float lo, float hi) {
  typedef __attribute__((ext_vector_type(2))) __bf16 bf16x2;
  f32x2 v = {lo, hi};
  return __builtin_bit_cast(unsigned, __builtin_convertvector(v, bf16x2));
}
template <int CTRL> __device__ __forceinline__ float dpp(float x) {
  return __builtin_bit_cast(float, __builtin_amdgcn_mov_dpp(__builtin_bit_cast(int, x), CTRL, 0xf, 0xf, true));
}
template <int I> __device__ __forceinline__ float rowb(float x) { return dpp<0x150 + I>(x); }
__device__ __forceinline__ float x16_sum(float x) {
  auto s = __builtin_amdgcn_permlane16_swap(__float_as_uint(x), __float_as_uint(x), false, false);
  return __uint_as_float(s[0]) + __uint_as_float(s[1]);
}
__device__ __forceinline__ float xh_max(float x) {
  auto s = __builtin_amdgcn_permlane16_swap(__float_as_uint(x), __float_as_uint(x), false, false);
  x = fmaxf(__uint_as_float(s[0]), __uint_as_float(s[1]));
  auto t = __builtin_amdgcn_permlane32_swap(__float_as_uint(x), __float_as_uint(x), false, false);
  return fmaxf(__uint_as_float(t[0]), __uint_as_float(t[1]));
}
__device__ __forceinline__ float max8(float x) { x = fmaxf(x, dpp<0xB1>(x)); x = fmaxf(x, dpp<0x4E>(x)); return fmaxf(x, dpp<0x141>(x)); }
__device__ __forceinline__ float sum8(float x) { x += dpp<0xB1>(x); x += dpp<0x4E>(x); return x + dpp<0x141>(x); }
__device__ __forceinline__ float sum16(float x) { x = sum8(x); return x + dpp<0x128>(x); }
__device__ __forceinline__ float lo16(unsigned x) { return __uint_as_float(x << 16); }
__device__ __forceinline__ float hi16(unsigned x) { return __uint_as_float(x & 0xFFFF0000u); }
__device__ __forceinline__ unsigned umax4(u32x4 v) { return max(max(v[0], v[1]), max(v[2], v[3])); }
#define RLX_AGENT __ATOMIC_RELAXED, __HIP_MEMORY_SCOPE_AGENT
#define RLX_WG __ATOMIC_RELAXED, __HIP_MEMORY_SCOPE_WORKGROUP
#define CFENCE() asm volatile("" ::: "memory")
#define SB() __builtin_amdgcn_sched_barrier(0)
#define WG_BARRIER() asm volatile("s_waitcnt lgkmcnt(0)\n\ts_barrier" ::: "memory")
#define PACE_TXT "1:\n\ts_memrealtime vcc\n\ts_waitcnt lgkmcnt(0)\n\ts_lshl_b32 vcc_lo, vcc_lo, 6\n\t" \
    "s_sub_i32 vcc_lo, vcc_lo, %0\n\ts_cmp_lt_i32 vcc_lo, 0\n\ts_cbranch_scc1 1b\n\ts_add_i32 %0, %0, %1"
#define PACE(t, d) asm volatile(PACE_TXT : "+s"(t) : "s"(d) : "vcc", "scc", "memory")
#define PACEC(t, d, thr, thr2) asm volatile(PACE_TXT "\n\ts_cmpk_ge_i32 vcc_lo, %3\n\ts_cbranch_scc1 3f\n\ts_cmpk_ge_i32 vcc_lo, %2\n\ts_cbranch_scc1 2f\n\t" \
    "s_waitcnt vmcnt(4)\n2:\n\ts_waitcnt vmcnt(5)\n3:\n\ts_waitcnt vmcnt(6)" : "+s"(t) : "s"(d), "i"(thr), "i"(thr2) : "vcc", "scc", "memory")
#define PACEE(t, d, thr, thr2) asm volatile(PACE_TXT "\n\ts_cmp_ge_i32 vcc_lo, %3\n\ts_cbranch_scc1 3f\n\ts_cmpk_ge_i32 vcc_lo, %2\n\ts_cbranch_scc1 2f\n\t" \
    "s_waitcnt vmcnt(4)\n2:\n\ts_waitcnt vmcnt(5)\n3:\n\ts_waitcnt vmcnt(6)" : "+s"(t) : "s"(d), "i"(thr), "s"(thr2) : "vcc", "scc", "memory")
#define ORD(i) (((i) & 3) == 0 ? 1 : ((i) & 3) == 1 ? 3 : ((i) & 3) == 2 ? 2 : 4)
#define THR(i) (LAGL * 16 * ORD(i))
#define THR2(i) (LAG2 * 64 + GR2 * 16 * ORD(i))
#define NOW64() ((unsigned)__builtin_amdgcn_s_memrealtime() << 6)
#define NAP(t) do { const unsigned t_ = (t); while ((int)(NOW64() - t_) < 0) __builtin_amdgcn_s_sleep(4); } while (0)

__global__ void __launch_bounds__(WAVES * 64)
tree_attn_decode(const unsigned short*, const unsigned short*, const unsigned short*, const unsigned short*, const unsigned short*,
                 const unsigned short*, unsigned short*, unsigned char*) {
  u64x8 args;
  unsigned long long tb64;
  asm volatile("s_load_dwordx16 %0, %2, 0x0\n\ts_memrealtime %1" : "=&s"(args), "=&s"(tb64) : "s"(__builtin_amdgcn_kernarg_segment_ptr()));
  SB();
  __shared__ __attribute__((aligned(16))) float lds[2048 + 1024 + 8];
  float* lds_o = lds + 2048;
  float* lds_m = lds_o + 1024;
  STAMP_DECL
  STAMP(0)
  const int bid = blockIdx.x, g = bid >> 3, s = bid & 7;
  const int tid = threadIdx.x, lane = tid & 63, wave = __builtin_amdgcn_readfirstlane(tid >> 6);
  const int r = lane & 15, h = lane >> 4;
  if (wave >= 4) {
    unsigned long long pc;
    unsigned j0, j1, j2;
    asm volatile("s_getpc_b64 %0" : "=s"(pc));
    pc &= ~255ull;
    asm volatile("s_load_dword %0, %3, 0x0\n\ts_load_dword %1, %3, 0x80\n\ts_load_dword %2, %4, 0x1000\n\ts_waitcnt lgkmcnt(0)"
                 : "=&s"(j0), "=&s"(j1), "=&s"(j2) : "s"(pc + g * 256), "s"(pc) : "memory");
  }
  const unsigned kvo = (r >> 2) * 256 + h * 64 + (r & 3) * 16, vvo = lane * 16;
  unsigned c0 = 0;
#pragma unroll
  for (int b = 0; b < 2; ++b) c0 ^= ((wave >> b) & 1) ? PCOL[6 + b] : 0u;
#pragma unroll
  for (int b = 0; b < 8; ++b) c0 ^= ((bid >> b) & 1) ? PCOL[8 + b] : 0u;
  const unsigned reg = ((c0 & 0x7FF) << 10) | (g << 21);
#define LOFF(i) (((((i) & 1) ? PCOL[0] : 0u) ^ (((i) & 2) ? PCOL[1] : 0u) ^ (((i) & 4) ? PCOL[2] : 0u)) << 10)
#define BOFF(b) (reg ^ (((((b) & 1) ? PCOL[3] : 0u) ^ (((b) & 2) ? PCOL[4] : 0u) ^ (((b) & 4) ? PCOL[5] : 0u)) << 10))
  const unsigned t0 = ((wave * 8 + (c0 >> 11)) * TOTAL >> 6) + HEAD * 64, dt = TOTAL / 2;
  const unsigned qo = (g * 2 + ((r >> 2) & 1)) * 256 + (4 * h + (r & 3)) * 16;
  asm volatile("" :: "s"(t0), "s"(reg), "v"(kvo), "v"(qo));
  SB();
  asm volatile("s_waitcnt lgkmcnt(0)" : "+s"(args), "+s"(tb64));
  const gu16* q = (const gu16*)args[0];
  gu8* ws = (gu8*)args[7];
  unsigned tb = (unsigned)tb64 << 6;
  if (INJS) tb += ((((s + 8 * (tb >> 18)) * 2654435761u) >> 16) % (unsigned)(INJS + 1)) * 64;
  if (wave >= 4) {
    const __amdgpu_buffer_rsrc_t rw = __builtin_amdgcn_make_buffer_rsrc((void*)ws, 0, WS_END, 0x00020000);
    lds_m[lane & 7] = __uint_as_float(EMPTY);
    WG_BARRIER();
    NAP(tb + 250 * 64);
    const unsigned tag = __hip_atomic_load((gu32*)(ws + WS_EP + g * 4352), RLX_AGENT) + 1u;
    __builtin_amdgcn_raw_buffer_store_b128(u32x4{EMPTY, EMPTY, EMPTY, EMPTY}, rw, lane * 16, WS_O + bid * 1024, AUX_SC1);
    float st = 0.f;
    u32x2 vv = {0u, 0u};
    if (s == 0) {
      const u32x2 kk = *(const GAS u32x2*)((const gu16*)args[3] + g * HD + 4 * (lane & 31));
      const u32x2 qq = *(const GAS u32x2*)(q + g * 2 * HD + 4 * lane);
      vv = *(const GAS u32x2*)((const gu16*)args[4] + g * HD + 4 * (lane & 31));
      const float bias = (1.0f - lo16(((const gu16*)args[5])[g / HKV])) * -1000000000.0f;
      st = (lo16(kk[0]) * lo16(qq[0]) + hi16(kk[0]) * hi16(qq[0])) + (lo16(kk[1]) * lo16(qq[1]) + hi16(kk[1]) * hi16(qq[1]));
      st = (x16_sum(sum16(st)) * SCALE + bias) * LOG2E;
    }
    asm volatile("s_waitcnt vmcnt(0)" ::: "memory");
    f32x4 w4;
    do {
      __builtin_amdgcn_s_sleep(8);
      CFENCE();
      w4 = *reinterpret_cast<const f32x4*>(lds_m + (lane >> 5) * 4);
    } while (__builtin_amdgcn_uicmp(umax4(__builtin_bit_cast(u32x4, w4)), EMPTY, 32));
    CFENCE();
    STAMP(1)
    const float M = fmaxf(fmaxf(w4[0], w4[1]), fmaxf(w4[2], w4[3]));
#pragma unroll
    for (int c = 0; c < 4; ++c) w4[c] = __builtin_amdgcn_exp2f(w4[c] - M);
    float L = 0.f;
#pragma unroll 1
    for (int k = 0; k < 8; ++k) {
      const f32x4 x = *reinterpret_cast<const f32x4*>(lds + (lane >> 5) * 1024 + k * 128 + (lane & 31) * 4);
      L += (__builtin_amdgcn_exp2f(fmaf(x[0], C2, -M)) + __builtin_amdgcn_exp2f(fmaf(x[1], C2, -M))) +
           (__builtin_amdgcn_exp2f(fmaf(x[2], C2, -M)) + __builtin_amdgcn_exp2f(fmaf(x[3], C2, -M)));
    }
    L = x16_sum(sum16(L));
#define REC(i) (*reinterpret_cast<const f32x4*>(lds_o + (i) * 2 * HD + 4 * lane))
    if (s != 0) {
      const u32x4 gm = {(lane & 1) ? tb : __float_as_uint(M), tag, __float_as_uint(L), tag};
      __builtin_amdgcn_raw_buffer_store_b128(gm, rw, (lane & 24) == 0 ? (lane >> 5) * 128 + (lane & 7) * 16 : OOB, WS_ML + bid * 256, AUX_SC1);
      STAMP(3)
      WG_BARRIER();
      STAMP(4)
      u32x4 go = __builtin_bit_cast(u32x4, (w4[0] * REC(0) + w4[1] * REC(1)) + (w4[2] * REC(2) + w4[3] * REC(3)));
#pragma unroll
      for (int c = 0; c < 4; ++c) go[c] = min(go[c], EMPTY - 1u);
      __builtin_amdgcn_raw_buffer_store_b128(go, rw, lane * 16, WS_O + bid * 1024, AUX_SC1);
    } else {
      NAP(NOW64() + 150 * 64);
      u32x4 a;
      for (;;) {
        CFENCE();
        a = __builtin_amdgcn_raw_buffer_load_b128(rw, (lane & 7) * 256 + (lane >> 5) * 128 + (lane & 8) * 2, WS_ML + bid * 256, AUX_SC1);
        if (((__builtin_amdgcn_uicmp(a[1], tag, 33) | __builtin_amdgcn_uicmp(a[3], tag, 33)) & 0xFEFEFEFEFEFEFEFEull) == 0) break;
        __builtin_amdgcn_s_sleep(16);
      }
      if (lane == 0) __hip_atomic_store((gu32*)(ws + WS_EP + g * 4352), tag, RLX_AGENT);
      const float mk = (lane & 7) == 0 ? M : __uint_as_float(a[0]), lk = (lane & 7) == 0 ? L : __uint_as_float(a[2]);
      const float Mx = max8(fmaxf(mk, st));
      const float wk = __builtin_amdgcn_exp2f(mk - Mx), wt = __builtin_amdgcn_exp2f(st - Mx);
      const float inv = 1.0f / (sum8(wk * lk) + wt), ck = wk * inv;
      w4 *= rowb<0>(ck);
      const float c1 = rowb<1>(ck), c2 = rowb<2>(ck), c3 = rowb<3>(ck), c4 = rowb<4>(ck), c5 = rowb<5>(ck), c6 = rowb<6>(ck), c7 = rowb<7>(ck);
      f32x4 o = rowb<0>(wt * inv) * f32x4{lo16(vv[0]), hi16(vv[0]), lo16(vv[1]), hi16(vv[1])};
      const float skew = max8((lane & 7) == 0 ? 0.f : (float)(int)(a[0] - tb));
      const unsigned te = tb + (HEAD + TOTAL + PDELAY) * 64 + __builtin_amdgcn_readlane((int)skew, 8);
      gu64* op = (gu64*)((gu16*)args[6] + g * 2 * HD + 4 * lane);
      STAMP(3)
      WG_BARRIER();
      STAMP(4)
      unsigned tp = NOW64() + POWN * 64;
      o += (w4[0] * REC(0) + w4[1] * REC(1)) + (w4[2] * REC(2) + w4[3] * REC(3));
      if ((int)(te - tp) > 0) tp = te;
      PACE(tp, dt);
      u32x4 po[NSPLIT];
      unsigned mx;
      do {
        CFENCE();
#pragma unroll
        for (int k = 1; k < NSPLIT; ++k) po[k] = __builtin_amdgcn_raw_buffer_load_b128(rw, lane * 16 + (k & 3) * 1024, WS_O + bid * 1024 + (k >> 2) * 4096, AUX_SC1);
        mx = umax4(po[1]);
#pragma unroll
        for (int k = 2; k < NSPLIT; ++k) mx = max(mx, umax4(po[k]));
      } while (__builtin_amdgcn_uicmp(mx, EMPTY, 32));
#define PO(k) __builtin_bit_cast(f32x4, po[k])
      o = ((o + c1 * PO(1)) + (c2 * PO(2) + c3 * PO(3))) + ((c4 * PO(4) + c5 * PO(5)) + (c6 * PO(6) + c7 * PO(7)));
      __hip_atomic_store(op, (unsigned long long)cvtpk(o[0], o[1]) | ((unsigned long long)cvtpk(o[2], o[3]) << 32), RLX_AGENT);
      if (DPROBE) { asm volatile("s_waitcnt vmcnt(0)" ::: "memory"); const unsigned until = tb + 3000 * 64 + 5 * (NOW64() - tb - 1800 * 64); while ((int)(NOW64() - until) < 0) __builtin_amdgcn_s_sleep(1); }
    }
    STAMP(5)
  } else {
    if (INJC | INJR | INJS) { const unsigned dl = 25 + INJC + (INJR ? (((bid + (tb >> 18)) * 2654435761u) >> 16) % (unsigned)(INJR + 1) : 0u); while ((int)(NOW64() - tb) < (int)dl * 64) {} }
    const __amdgpu_buffer_rsrc_t rk = __builtin_amdgcn_make_buffer_rsrc((void*)args[1], 0, NGRP * CTX * HD * 2, 0x00020000);
    const __amdgpu_buffer_rsrc_t rv = __builtin_amdgcn_make_buffer_rsrc((void*)args[2], 0, NGRP * CTX * HD * 2, 0x00020000);
    unsigned tn = tb + t0 + NSKIP * dt;
    u32x4 buf[8], qf;
#pragma unroll
    for (int i = 0; i < 8; ++i) {
      if (i >= 5) PACEC(tn, dt, THR(i), THR2(i)); else if (i >= NSKIP) PACE(tn, dt);
      buf[i] = __builtin_amdgcn_raw_buffer_load_b128(rk, kvo, reg ^ LOFF(i), AUX_NT);
      SB();
      if (i == 0) { qf = *(const GAS u32x4*)((const gu8*)q + qo); SB(); }
      if (i == 7) __builtin_amdgcn_s_barrier();
    }
    const int jj = (r & 3) + 4 * (r >> 3);
    unsigned msk[4];
#pragma unroll
    for (int c = 0; c < 4; ++c) msk[c] = (c == (jj >> 1)) ? ((jj & 1) ? 0xFFFF0000u : 0x0000FFFFu) : 0u;
    lf32* mp = (lf32*)(lds_m + (lane & 4) + wave);
    asm volatile("" : "+v"(msk[0]), "+v"(msk[1]), "+v"(msk[2]), "+v"(msk[3]), "+v"(mp));
    float* sp = lds + (((lane >> 2) & 1) * 4 + wave) * 256 + h * 8;
    float m = -INFINITY;
#pragma unroll 1
    for (int b = 1; b <= 8; ++b) {
      const __amdgpu_buffer_rsrc_t rs = __builtin_amdgcn_make_buffer_rsrc((void*)(b < 8 ? args[1] : args[2]), 0, NGRP * CTX * HD * 2, 0x00020000);
      const unsigned vo = b < 8 ? kvo : vvo, bo = BOFF(b);
      float S[8];
#pragma unroll
      for (int i = 0; i < 8; ++i) {
        const f32x4 z = {0.f, 0.f, 0.f, 0.f};
        const f32x4 d = __builtin_amdgcn_mfma_f32_16x16x32_bf16(__builtin_bit_cast(bf16x8, buf[i]), __builtin_bit_cast(bf16x8, qf), z, 0, 0, 0);
        S[i] = (dpp<0x00>(d[0]) + dpp<0x55>(d[1])) + (dpp<0xAA>(d[2]) + dpp<0xFF>(d[3]));
        m = fmaxf(m, S[i]);
        asm volatile("" : "+v"(m));
        if ((i & 3) == 3) *reinterpret_cast<f32x4*>(sp + (b - 1) * 32 + (i & 4)) = f32x4{S[i - 3], S[i - 2], S[i - 1], S[i]};
        SB();
        PACEC(tn, dt, THR(i), THR2(i));
        buf[i] = __builtin_amdgcn_raw_buffer_load_b128(rs, vo, bo ^ LOFF(i), AUX_NT);
        SB();
      }
    }
    f32x4 s0 = *reinterpret_cast<const f32x4*>(sp), s1 = *reinterpret_cast<const f32x4*>(sp + 4);
    const float mc = xh_max(m) * C2;
    CFENCE();
    *mp = __uint_as_float(min(__float_as_uint(mc), EMPTY - 1u));
    f32x4 acc = {0.f, 0.f, 0.f, 0.f};
#define PV(sc, i) do { const float p_ = __builtin_amdgcn_exp2f(fmaf(sc, C2, -mc)); const unsigned pp_ = cvtpk(p_, p_); \
      const u32x4 a_ = {pp_ & msk[0], pp_ & msk[1], pp_ & msk[2], pp_ & msk[3]}; \
      acc = __builtin_amdgcn_mfma_f32_16x16x32_bf16(__builtin_bit_cast(bf16x8, a_), __builtin_bit_cast(bf16x8, buf[i]), acc, 0, 0, 0); SB(); } while (0)
#pragma unroll 1
    for (int b = 1; b < 8; ++b) {
      const unsigned bo = BOFF(b);
      const int te = b == 7 ? 0 : (LAG2 + GR2) * 64;
#pragma unroll
      for (int i = 0; i < 8; ++i) {
        PV(i < 4 ? s0[i & 3] : s1[i & 3], i);
        if (i == 3) s0 = *reinterpret_cast<const f32x4*>(sp + b * 32);
        if (i == 7) s1 = *reinterpret_cast<const f32x4*>(sp + b * 32 + 4);
        if (i >= 8 - NFREE) PACEE(tn, dt, THR(i), te); else PACEC(tn, dt, THR(i), THR2(i));
        buf[i] = __builtin_amdgcn_raw_buffer_load_b128(rv, vvo, bo ^ LOFF(i), AUX_NT);
        SB();
      }
    }
#pragma unroll
    for (int i = 0; i < 8; ++i) PV(i < 4 ? s0[i & 3] : s1[i & 3], i);
    *reinterpret_cast<f32x4*>(lds_o + wave * 2 * HD + (h & 1) * HD + 8 * r + 4 * (h >> 1)) = acc;
    WG_BARRIER();
  }
  STAMP_OUT
}

extern "C" void kernel_launch(void* const* d_in, const int* in_sizes, int n_in, void* d_out, int out_size, void* d_ws, size_t ws_size,
                              hipStream_t stream) {
  tree_attn_decode<<<NGRP * NSPLIT, WAVES * 64, 0, stream>>>((const unsigned short*)d_in[0], (const unsigned short*)d_in[1], (const unsigned short*)d_in[2],
                                                             (const unsigned short*)d_in[3], (const unsigned short*)d_in[4], (const unsigned short*)d_in[5],
                                                             (unsigned short*)d_out, (unsigned char*)d_ws);
}
```

```cpp
#include <hip/hip_runtime.h>
#include <hip/hip_bf16.h>
#include <stdint.h>

typedef __attribute__((ext_vector_type(8))) __bf16 bf16x8;
typedef __attribute__((ext_vector_type(4))) float f32x4;
typedef __attribute__((ext_vector_type(2))) float f32x2;
typedef __attribute__((ext_vector_type(4))) unsigned u32x4;
typedef __attribute__((ext_vector_type(2))) unsigned u32x2;
typedef __attribute__((ext_vector_type(8))) unsigned long long u64x8;
#define GAS __attribute__((address_space(1)))
typedef GAS unsigned char gu8;
typedef GAS unsigned short gu16;
typedef GAS unsigned gu32;
typedef GAS unsigned long long gu64;
typedef __attribute__((address_space(3))) float lf32;

#define TOTAL 1716
#define HEAD 25
#define PDELAY 90
#define POWN 15
#define LAGL 40
#define LAG2 40
#define GR2 40
#define NSKIP 2
#define NFREE 4
#define DPROBE 0
#define INJC 0
#define INJR 0
#define INJS 0
#define PATC 0x40, 0x66f, 0x6d4, 0x4e5, 0x200, 0xe1, 0x2a5, 0x28, 0x2948, 0x1029, 0x33cd, 0x829, 0xc9d, 0x46c, 0x805, 0x78f
#define SPFV 1
#define STAMP(i)
#define STAMP_DECL
#define STAMP_OUT

constexpr int NB = 2, HQ = 32, HKV = 16, CTX = 8192, HD = 128;
constexpr int NGRP = NB * HKV;
constexpr int NSPLIT = 8, WAVES = 5;
constexpr float SCALE = 0.088388347648318440f, LOG2E = 1.4426950408889634f;
constexpr float C2 = SCALE * LOG2E;
constexpr int WS_O = 0, WS_ML = 262144, WS_EP = WS_ML + 65536, WS_END = WS_EP + NGRP * 4352;
constexpr int AUX_NT = 2, AUX_SC1 = 16;
constexpr unsigned PCOL[16] = {PATC};
constexpr unsigned OOB = 0x80000000u, EMPTY = 0xFFFFFFFFu;

__device__ __forceinline__ unsigned cvtpk(float lo, float hi) {
  typedef __attribute__((ext_vector_type(2))) __bf16 bf16x2;
  f32x2 v = {lo, hi};
  return __builtin_bit_cast(unsigned, __builtin_convertvector(v, bf16x2));
}
template <int CTRL> __device__ __forceinline__ float dpp(float x) {
  return __builtin_bit_cast(float, __builtin_amdgcn_mov_dpp(__builtin_bit_cast(int, x), CTRL, 0xf, 0xf, true));
}
template <int I> __device__ __forceinline__ float rowb(float x) { return dpp<0x150 + I>(x); }
__device__ __forceinline__ float x16_sum(float x) {
  auto s = __builtin_amdgcn_permlane16_swap(__float_as_uint(x), __float_as_uint(x), false, false);
  return __uint_as_float(s[0]) + __uint_as_float(s[1]);
}
__device__ __forceinline__ float xh_max(float x) {
  auto s = __builtin_amdgcn_permlane16_swap(__float_as_uint(x), __float_as_uint(x), false, false);
  x = fmaxf(__uint_as_float(s[0]), __uint_as_float(s[1]));
  auto t = __builtin_amdgcn_permlane32_swap(__float_as_uint(x), __float_as_uint(x), false, false);
  return fmaxf(__uint_as_float(t[0]), __uint_as_float(t[1]));
}
__device__ __forceinline__ float max8(float x) { x = fmaxf(x, dpp<0xB1>(x)); x = fmaxf(x, dpp<0x4E>(x)); return fmaxf(x, dpp<0x141>(x)); }
__device__ __forceinline__ float sum8(float x) { x += dpp<0xB1>(x); x += dpp<0x4E>(x); return x + dpp<0x141>(x); }
__device__ __forceinline__ float sum16(float x) { x = sum8(x); return x + dpp<0x128>(x); }
__device__ __forceinline__ float lo16(unsigned x) { return __uint_as_float(x << 16); }
__device__ __forceinline__ float hi16(unsigned x) { return __uint_as_float(x & 0xFFFF0000u); }
__device__ __forceinline__ unsigned umax4(u32x4 v) { return max(max(v[0], v[1]), max(v[2], v[3])); }
#define RLX_AGENT __ATOMIC_RELAXED, __HIP_MEMORY_SCOPE_AGENT
#define RLX_WG __ATOMIC_RELAXED, __HIP_MEMORY_SCOPE_WORKGROUP
#define CFENCE() asm volatile("" ::: "memory")
#define SB() __builtin_amdgcn_sched_barrier(0)
#define WG_BARRIER() asm volatile("s_waitcnt lgkmcnt(0)\n\ts_barrier" ::: "memory")
#define PACE_TXT "1:\n\ts_memrealtime vcc\n\ts_waitcnt lgkmcnt(0)\n\ts_lshl_b32 vcc_lo, vcc_lo, 6\n\t" \
    "s_sub_i32 vcc_lo, vcc_lo, %0\n\ts_cmp_lt_i32 vcc_lo, 0\n\ts_cbranch_scc1 1b\n\ts_add_i32 %0, %0, %1"
#define PACE(t, d) asm volatile(PACE_TXT : "+s"(t) : "s"(d) : "vcc", "scc", "memory")
#define PACEC(t, d, thr, thr2) asm volatile(PACE_TXT "\n\ts_cmpk_ge_i32 vcc_lo, %3\n\ts_cbranch_scc1 3f\n\ts_cmpk_ge_i32 vcc_lo, %2\n\ts_cbranch_scc1 2f\n\t" \
    "s_waitcnt vmcnt(4)\n2:\n\ts_waitcnt vmcnt(5)\n3:\n\ts_waitcnt vmcnt(6)" : "+s"(t) : "s"(d), "i"(thr), "i"(thr2) : "vcc", "scc", "memory")
#define PACEE(t, d, thr, thr2) asm volatile(PACE_TXT "\n\ts_cmp_ge_i32 vcc_lo, %3\n\ts_cbranch_scc1 3f\n\ts_cmpk_ge_i32 vcc_lo, %2\n\ts_cbranch_scc1 2f\n\t" \
    "s_waitcnt vmcnt(4)\n2:\n\ts_waitcnt vmcnt(5)\n3:\n\ts_waitcnt vmcnt(6)" : "+s"(t) : "s"(d), "i"(thr), "s"(thr2) : "vcc", "scc", "memory")
#define ORD(i) (((i) & 3) == 0 ? 1 : ((i) & 3) == 1 ? 3 : ((i) & 3) == 2 ? 2 : 4)
#define THR(i) (LAGL * 16 * ORD(i))
#define THR2(i) (LAG2 * 64 + GR2 * 16 * ORD(i))
#define NOW64() ((unsigned)__builtin_amdgcn_s_memrealtime() << 6)
#define NAP(t) do { const unsigned t_ = (t); while ((int)(NOW64() - t_) < 0) __builtin_amdgcn_s_sleep(4); } while (0)

__global__ void __launch_bounds__(WAVES * 64)
tree_attn_decode(const unsigned short*, const unsigned short*, const unsigned short*, const unsigned short*, const unsigned short*,
                 const unsigned short*, unsigned short*, unsigned char*) {
  u64x8 args;
  unsigned long long tb64;
  asm volatile("s_load_dwordx16 %0, %2, 0x0\n\ts_memrealtime %1" : "=&s"(args), "=&s"(tb64) : "s"(__builtin_amdgcn_kernarg_segment_ptr()));
  SB();
  __shared__ __attribute__((aligned(16))) float lds[2048 + 1024 + 8 + 16];
  float* lds_o = lds + 2048;
  float* lds_m = lds_o + 1024;
  STAMP_DECL
  STAMP(0)
  const int bid = blockIdx.x, g = bid >> 3, s = bid & 7;
  const int tid = threadIdx.x, lane = tid & 63, wave = __builtin_amdgcn_readfirstlane(tid >> 6);
  const int r = lane & 15, h = lane >> 4;
  if (wave >= 4) {
    unsigned long long pc;
    unsigned j0, j1, j2;
    lds_m[lane & 7] = __uint_as_float(EMPTY);
    asm volatile("s_getpc_b64 %0" : "=s"(pc));
    pc &= ~255ull;
    asm volatile("s_waitcnt lgkmcnt(0)\n\ts_load_dword %0, %3, 0x0\n\ts_load_dword %1, %3, 0x80\n\ts_load_dword %2, %4, 0x1000\n\ts_barrier\n\ts_waitcnt lgkmcnt(0)"
                 : "=&s"(j0), "=&s"(j1), "=&s"(j2) : "s"(pc + g * 256), "s"(pc) : "memory");
  }
  const unsigned kvo = (r >> 2) * 256 + h * 64 + (r & 3) * 16, vvo = lane * 16;
  unsigned c0 = 0;
#pragma unroll
  for (int b = 0; b < 2; ++b) c0 ^= ((wave >> b) & 1) ? PCOL[6 + b] : 0u;
#pragma unroll
  for (int b = 0; b < 8; ++b) c0 ^= ((bid >> b) & 1) ? PCOL[8 + b] : 0u;
  const unsigned reg = ((c0 & 0x7FF) << 10) | (g << 21);
#define LOFF(i) (((((i) & 1) ? PCOL[0] : 0u) ^ (((i) & 2) ? PCOL[1] : 0u) ^ (((i) & 4) ? PCOL[2] : 0u)) << 10)
#define BOFF(b) (reg ^ (((((b) & 1) ? PCOL[3] : 0u) ^ (((b) & 2) ? PCOL[4] : 0u) ^ (((b) & 4) ? PCOL[5] : 0u)) << 10))
  const unsigned t0 = ((wave * 8 + (c0 >> 11)) * TOTAL >> 6) + HEAD * 64, dt = TOTAL / 2;
  const unsigned qo = (g * 2 + ((r >> 2) & 1)) * 256 + (4 * h + (r & 3)) * 16;
  asm volatile("" :: "s"(t0), "s"(reg), "v"(kvo), "v"(qo));
  SB();
  asm volatile("s_waitcnt lgkmcnt(0)" : "+s"(args), "+s"(tb64));
  const gu16* q = (const gu16*)args[0];
  gu8* ws = (gu8*)args[7];
  unsigned tb = (unsigned)tb64 << 6;
  if (INJS) tb += ((((s + 8 * (tb >> 18)) * 2654435761u) >> 16) % (unsigned)(INJS + 1)) * 64;
  if (wave >= 4) {
    const __amdgpu_buffer_rsrc_t rw = __builtin_amdgcn_make_buffer_rsrc((void*)ws, 0, WS_END, 0x00020000);
    NAP(tb + 250 * 64);
    if (SPFV) { unsigned j0, j1; asm volatile("s_load_dword %0, %2, 0x0\n\ts_load_dword %1, %3, 0x0\n\ts_waitcnt lgkmcnt(0)" : "=&s"(j0), "=&s"(j1) : "s"(args[2] + reg), "s"(args[7] + bid * 1024) : "memory"); }
    if (SPFV == 2) { const unsigned jv = *(const volatile GAS unsigned*)(args[2] + reg); asm volatile("" :: "v"(jv)); }
    const unsigned tag = __hip_atomic_load((gu32*)(ws + WS_EP + g * 4352), RLX_AGENT) + 1u;
    __builtin_amdgcn_raw_buffer_store_b128(u32x4{EMPTY, EMPTY, EMPTY, EMPTY}, rw, lane * 16, WS_O + bid * 1024, AUX_SC1);
    float st = 0.f;
    u32x2 vv = {0u, 0u};
    if (s == 0) {
      const u32x2 kk = *(const GAS u32x2*)((const gu16*)args[3] + g * HD + 4 * (lane & 31));
      const u32x2 qq = *(const GAS u32x2*)(q + g * 2 * HD + 4 * lane);
      vv = *(const GAS u32x2*)((const gu16*)args[4] + g * HD + 4 * (lane & 31));
      const float bias = (1.0f - lo16(((const gu16*)args[5])[g / HKV])) * -1000000000.0f;
      st = (lo16(kk[0]) * lo16(qq[0]) + hi16(kk[0]) * hi16(qq[0])) + (lo16(kk[1]) * lo16(qq[1]) + hi16(kk[1]) * hi16(qq[1]));
      st = (x16_sum(sum16(st)) * SCALE + bias) * LOG2E;
    }
    asm volatile("s_waitcnt vmcnt(0)" ::: "memory");
    f32x4 w4;
    do {
      __builtin_amdgcn_s_sleep(8);
      CFENCE();
      w4 = *reinterpret_cast<const f32x4*>(lds_m + (lane >> 5) * 4);
    } while (__builtin_amdgcn_uicmp(umax4(__builtin_bit_cast(u32x4, w4)), EMPTY, 32));
    CFENCE();
    STAMP(1)
    const float M = fmaxf(fmaxf(w4[0], w4[1]), fmaxf(w4[2], w4[3]));
#pragma unroll
    for (int c = 0; c < 4; ++c) w4[c] = __builtin_amdgcn_exp2f(w4[c] - M);
    float L = 0.f;
#pragma unroll 1
    for (int k = 0; k < 8; ++k) {
      const f32x4 x = *reinterpret_cast<const f32x4*>(lds + (lane >> 5) * 1024 + k * 128 + (lane & 31) * 4);
      L += (__builtin_amdgcn_exp2f(fmaf(x[0], C2, -M)) + __builtin_amdgcn_exp2f(fmaf(x[1], C2, -M))) +
           (__builtin_amdgcn_exp2f(fmaf(x[2], C2, -M)) + __builtin_amdgcn_exp2f(fmaf(x[3], C2, -M)));
    }
    L = x16_sum(sum16(L));
#define REC(i) (*reinterpret_cast<const f32x4*>(lds_o + (i) * 2 * HD + 4 * lane))
    if (s != 0) {
      const u32x4 gm = {(lane & 1) ? tb : __float_as_uint(M), tag, __float_as_uint(L), tag};
      __builtin_amdgcn_raw_buffer_store_b128(gm, rw, (lane & 24) == 0 ? (lane >> 5) * 128 + (lane & 7) * 16 : OOB, WS_ML + bid * 256, AUX_SC1);
      STAMP(3)
      WG_BARRIER();
      STAMP(4)
      u32x4 go = __builtin_bit_cast(u32x4, (w4[0] * REC(0) + w4[1] * REC(1)) + (w4[2] * REC(2) + w4[3] * REC(3)));
#pragma unroll
      for (int c = 0; c < 4; ++c) go[c] = min(go[c], EMPTY - 1u);
      __builtin_amdgcn_raw_buffer_store_b128(go, rw, lane * 16, WS_O + bid * 1024, AUX_SC1);
    } else {
      NAP(NOW64() + 150 * 64);
      u32x4 a;
      for (;;) {
        CFENCE();
        a = __builtin_amdgcn_raw_buffer_load_b128(rw, (lane & 7) * 256 + (lane >> 5) * 128 + (lane & 8) * 2, WS_ML + bid * 256, AUX_SC1);
        if (((__builtin_amdgcn_uicmp(a[1], tag, 33) | __builtin_amdgcn_uicmp(a[3], tag, 33)) & 0xFEFEFEFEFEFEFEFEull) == 0) break;
        __builtin_amdgcn_s_sleep(16);
      }
      if (lane == 0) __hip_atomic_store((gu32*)(ws + WS_EP + g * 4352), tag, RLX_AGENT);
      const float mk = (lane & 7) == 0 ? M : __uint_as_float(a[0]), lk = (lane & 7) == 0 ? L : __uint_as_float(a[2]);
      const float Mx = max8(fmaxf(mk, st));
      const float wk = __builtin_amdgcn_exp2f(mk - Mx), wt = __builtin_amdgcn_exp2f(st - Mx);
      const float inv = 1.0f / (sum8(wk * lk) + wt), ck = wk * inv;
      w4 *= rowb<0>(ck);
      const float c1 = rowb<1>(ck), c2 = rowb<2>(ck), c3 = rowb<3>(ck), c4 = rowb<4>(ck), c5 = rowb<5>(ck), c6 = rowb<6>(ck), c7 = rowb<7>(ck);
      f32x4 o = rowb<0>(wt * inv) * f32x4{lo16(vv[0]), hi16(vv[0]), lo16(vv[1]), hi16(vv[1])};
      const float skew = max8((lane & 7) == 0 ? 0.f : (float)(int)(a[0] - tb));
      const unsigned te = tb + (HEAD + TOTAL + PDELAY) * 64 + __builtin_amdgcn_readlane((int)skew, 8);
      gu64* op = (gu64*)((gu16*)args[6] + g * 2 * HD + 4 * lane);
      STAMP(3)
      WG_BARRIER();
      STAMP(4)
      unsigned tp = NOW64() + POWN * 64;
      o += (w4[0] * REC(0) + w4[1] * REC(1)) + (w4[2] * REC(2) + w4[3] * REC(3));
      if ((int)(te - tp) > 0) tp = te;
      PACE(tp, dt);
      u32x4 po[NSPLIT];
      unsigned mx;
      do {
        CFENCE();
#pragma unroll
        for (int k = 1; k < NSPLIT; ++k) po[k] = __builtin_amdgcn_raw_buffer_load_b128(rw, lane * 16 + (k & 3) * 1024, WS_O + bid * 1024 + (k >> 2) * 4096, AUX_SC1);
        mx = umax4(po[1]);
#pragma unroll
        for (int k = 2; k < NSPLIT; ++k) mx = max(mx, umax4(po[k]));
      } while (__builtin_amdgcn_uicmp(mx, EMPTY, 32));
#define PO(k) __builtin_bit_cast(f32x4, po[k])
      o = ((o + c1 * PO(1)) + (c2 * PO(2) + c3 * PO(3))) + ((c4 * PO(4) + c5 * PO(5)) + (c6 * PO(6) + c7 * PO(7)));
      __hip_atomic_store(op, (unsigned long long)cvtpk(o[0], o[1]) | ((unsigned long long)cvtpk(o[2], o[3]) << 32), RLX_AGENT);
      if (DPROBE) { asm volatile("s_waitcnt vmcnt(0)" ::: "memory"); const unsigned until = tb + 3000 * 64 + 5 * (NOW64() - tb - 1800 * 64); while ((int)(NOW64() - until) < 0) __builtin_amdgcn_s_sleep(1); }
    }
    {
      const unsigned tend = NOW64();
      NAP(tb + 2600 * 64);
      gu32* dg = (gu32*)(ws + (1 << 20));
      if (lane < 16) __hip_atomic_store(dg + bid * 32 + lane, ((unsigned*)lds)[3080 + lane], RLX_AGENT);
      if (lane == 16) __hip_atomic_store(dg + bid * 32 + 16, tb, RLX_AGENT);
      if (lane == 17) __hip_atomic_store(dg + bid * 32 + 17, tend, RLX_AGENT);
      if (bid == 0) {
        NAP(tb + 3200 * 64);
        float sm = 0.f, mx = -1e9f;
        for (int k = 0; k < 64; ++k) { const float v = (float)(int)__hip_atomic_load(dg + ((lane >> 4) * 64 + k) * 32 + (lane & 15), RLX_AGENT); sm += v; mx = fmaxf(mx, v); }
        sm = x16_sum(sm); { auto t = __builtin_amdgcn_permlane32_swap(__float_as_uint(sm), __float_as_uint(sm), false, false); sm = __uint_as_float(t[0]) + __uint_as_float(t[1]); }
        mx = xh_max(mx);
        float st_ = 0.f, l15 = 0.f, en = -1e9f;
        for (int k = 0; k < 32; ++k) { const int bb = k * 8 + (lane & 7);
          st_ += (float)((int)(__hip_atomic_load(dg + bb * 32 + 16, RLX_AGENT) - tb) >> 6) * (1.f / 32); l15 += (float)(int)__hip_atomic_load(dg + bb * 32 + 15, RLX_AGENT) * (1.f / 32);
          en = fmaxf(en, (float)((int)(__hip_atomic_load(dg + bb * 32 + 17, RLX_AGENT) - tb) >> 6)); }
        unsigned long long pk[10];
        for (int w = 0; w < 10; ++w) pk[w] = 0;
#pragma unroll
        for (int k = 0; k < 16; ++k) { pk[k >> 3] |= (unsigned long long)(min(__builtin_amdgcn_readlane((int)(sm * (1.f / 256)), k), 255) & 255) << (8 * (7 - (k & 7)));
                                       pk[2 + (k >> 3)] |= (unsigned long long)(min(__builtin_amdgcn_readlane((int)mx, k), 255) & 255) << (8 * (7 - (k & 7))); }
#pragma unroll
        for (int k = 0; k < 8; ++k) { pk[4] |= (unsigned long long)((__builtin_amdgcn_readlane((int)st_, k) + 64) & 255) << (8 * (7 - k)); pk[5] |= (unsigned long long)(min(__builtin_amdgcn_readlane((int)l15, k), 255) & 255) << (8 * (7 - k));
                                      pk[6] |= (unsigned long long)(min(__builtin_amdgcn_readlane((int)en, k) - 1780, 255) & 255) << (8 * (7 - k)); }
        if (lane == 0) {
          auto msg = __ockl_fprintf_stderr_begin();
          msg = __ockl_fprintf_append_string_n(msg, "D %016lx%016lx %016lx%016lx %016lx %016lx %016lx\n", 50, 0);
          __ockl_fprintf_append_args(msg, 7, pk[0], pk[1], pk[2], pk[3], pk[4], pk[5], pk[6], 1);
          if (((tb >> 8) * 2654435761u >> 16) % 300 == 999) ((gu16*)args[6])[0] = 0x7000;
        }
      }
    }
  } else {
    if (INJC | INJR | INJS) { const unsigned dl = 25 + INJC + (INJR ? (((bid + (tb >> 18)) * 2654435761u) >> 16) % (unsigned)(INJR + 1) : 0u); while ((int)(NOW64() - tb) < (int)dl * 64) {} }
    const __amdgpu_buffer_rsrc_t rk = __builtin_amdgcn_make_buffer_rsrc((void*)args[1], 0, NGRP * CTX * HD * 2, 0x00020000);
    const __amdgpu_buffer_rsrc_t rv = __builtin_amdgcn_make_buffer_rsrc((void*)args[2], 0, NGRP * CTX * HD * 2, 0x00020000);
    unsigned tn = tb + t0 + NSKIP * dt;
    u32x4 buf[8], qf;
#pragma unroll
    for (int i = 0; i < 8; ++i) {
      if (i >= 5) PACEC(tn, dt, THR(i), THR2(i)); else if (i >= NSKIP) PACE(tn, dt);
      buf[i] = __builtin_amdgcn_raw_buffer_load_b128(rk, kvo, reg ^ LOFF(i), AUX_NT);
      SB();
      if (i == 0) { qf = *(const GAS u32x4*)((const gu8*)q + qo); SB(); }
      if (i == 2) __builtin_amdgcn_s_barrier();
    }
    if (tid == 0) ((int*)lds)[3080 + (0)] = (int)(NOW64() - (tn - dt)) >> 6;
    const int jj = (r & 3) + 4 * (r >> 3);
    unsigned msk[4];
#pragma unroll
    for (int c = 0; c < 4; ++c) msk[c] = (c == (jj >> 1)) ? ((jj & 1) ? 0xFFFF0000u : 0x0000FFFFu) : 0u;
    lf32* mp = (lf32*)(lds_m + (lane & 4) + wave);
    asm volatile("" : "+v"(msk[0]), "+v"(msk[1]), "+v"(msk[2]), "+v"(msk[3]), "+v"(mp));
    float* sp = lds + (((lane >> 2) & 1) * 4 + wave) * 256 + h * 8;
    float m = -INFINITY;
#pragma unroll 1
    for (int b = 1; b <= 8; ++b) {
      const __amdgpu_buffer_rsrc_t rs = __builtin_amdgcn_make_buffer_rsrc((void*)(b < 8 ? args[1] : args[2]), 0, NGRP * CTX * HD * 2, 0x00020000);
      const unsigned vo = b < 8 ? kvo : vvo, bo = BOFF(b);
      float S[8];
#pragma unroll
      for (int i = 0; i < 8; ++i) {
        const f32x4 z = {0.f, 0.f, 0.f, 0.f};
        const f32x4 d = __builtin_amdgcn_mfma_f32_16x16x32_bf16(__builtin_bit_cast(bf16x8, buf[i]), __builtin_bit_cast(bf16x8, qf), z, 0, 0, 0);
        S[i] = (dpp<0x00>(d[0]) + dpp<0x55>(d[1])) + (dpp<0xAA>(d[2]) + dpp<0xFF>(d[3]));
        m = fmaxf(m, S[i]);
        asm volatile("" : "+v"(m));
        if ((i & 3) == 3) *reinterpret_cast<f32x4*>(sp + (b - 1) * 32 + (i & 4)) = f32x4{S[i - 3], S[i - 2], S[i - 1], S[i]};
        SB();
        PACEC(tn, dt, THR(i), THR2(i));
        buf[i] = __builtin_amdgcn_raw_buffer_load_b128(rs, vo, bo ^ LOFF(i), AUX_NT);
        SB();
        if (i == 5) { if (tid == 0) ((int*)lds)[3080 + (b)] = (int)(NOW64() - (tn - dt)) >> 6; SB(); }
      }
    }
    f32x4 s0 = *reinterpret_cast<const f32x4*>(sp), s1 = *reinterpret_cast<const f32x4*>(sp + 4);
    const float mc = xh_max(m) * C2;
    CFENCE();
    *mp = __uint_as_float(min(__float_as_uint(mc), EMPTY - 1u));
    f32x4 acc = {0.f, 0.f, 0.f, 0.f};
#define PV(sc, i) do { const float p_ = __builtin_amdgcn_exp2f(fmaf(sc, C2, -mc)); const unsigned pp_ = cvtpk(p_, p_); \
      const u32x4 a_ = {pp_ & msk[0], pp_ & msk[1], pp_ & msk[2], pp_ & msk[3]}; \
      acc = __builtin_amdgcn_mfma_f32_16x16x32_bf16(__builtin_bit_cast(bf16x8, a_), __builtin_bit_cast(bf16x8, buf[i]), acc, 0, 0, 0); SB(); } while (0)
#pragma unroll 1
    for (int b = 1; b < 8; ++b) {
      const unsigned bo = BOFF(b);
      const int te = b == 7 ? 0 : (LAG2 + GR2) * 64;
#pragma unroll
      for (int i = 0; i < 8; ++i) {
        PV(i < 4 ? s0[i & 3] : s1[i & 3], i);
        if (i == 3) s0 = *reinterpret_cast<const f32x4*>(sp + b * 32);
        if (i == 7) s1 = *reinterpret_cast<const f32x4*>(sp + b * 32 + 4);
        if (i >= 8 - NFREE) PACEE(tn, dt, THR(i), te); else PACEC(tn, dt, THR(i), THR2(i));
        buf[i] = __builtin_amdgcn_raw_buffer_load_b128(rv, vvo, bo ^ LOFF(i), AUX_NT);
        SB();
        if (i == 2) { if (tid == 0) ((int*)lds)[3080 + (8 + b)] = (int)(NOW64() - (tn - dt)) >> 6; SB(); }
      }
    }
#pragma unroll
    for (int i = 0; i < 8; ++i) PV(i < 4 ? s0[i & 3] : s1[i & 3], i);
    *reinterpret_cast<f32x4*>(lds_o + wave * 2 * HD + (h & 1) * HD + 8 * r + 4 * (h >> 1)) = acc;
    WG_BARRIER();
  }
  STAMP_OUT
}

extern "C" void kernel_launch(void* const* d_in, const int* in_sizes, int n_in, void* d_out, int out_size, void* d_ws, size_t ws_size,
                              hipStream_t stream) {
  tree_attn_decode<<<NGRP * NSPLIT, WAVES * 64, 0, stream>>>((const unsigned short*)d_in[0], (const unsigned short*)d_in[1], (const unsigned short*)d_in[2],
                                                             (const unsigned short*)d_in[3], (const unsigned short*)d_in[4], (const unsigned short*)d_in[5],
                                                             (unsigned short*)d_out, (unsigned char*)d_ws);
}
```

```cpp
#include <hip/hip_runtime.h>
#include <hip/hip_bf16.h>
#include <stdint.h>

typedef __attribute__((ext_vector_type(8))) __bf16 bf16x8;
typedef __attribute__((ext_vector_type(4))) float f32x4;
typedef __attribute__((ext_vector_type(2))) float f32x2;
typedef __attribute__((ext_vector_type(4))) unsigned u32x4;
typedef __attribute__((ext_vector_type(2))) unsigned u32x2;
typedef __attribute__((ext_vector_type(8))) unsigned long long u64x8;
#define GAS __attribute__((address_space(1)))
typedef GAS unsigned char gu8;
typedef GAS unsigned short gu16;
typedef GAS unsigned gu32;
typedef GAS unsigned long long gu64;
typedef __attribute__((address_space(3))) float lf32;

#define TOTAL 1706
#define HEAD 0
#define PDELAY 90
#define POWN 15
#define LAGL 40
#define LAG2 40
#define GR2 40
#define NSKIP 2
#define NFREE 4
#define DPROBE 0
#define INJC 0
#define INJR 0
#define INJS 0
#define PATC 0x40, 0x66f, 0x6d4, 0x4e5, 0x200, 0xe1, 0x2a5, 0x28, 0x2948, 0x1029, 0x33cd, 0x829, 0xc9d, 0x46c, 0x805, 0x78f
#define SPFV 0
#define STAMP(i)
#define STAMP_DECL
#define STAMP_OUT

constexpr int NB = 2, HQ = 32, HKV = 16, CTX = 8192, HD = 128;
constexpr int NGRP = NB * HKV;
constexpr int NSPLIT = 8, WAVES = 5;
constexpr float SCALE = 0.088388347648318440f, LOG2E = 1.4426950408889634f;
constexpr float C2 = SCALE * LOG2E;
constexpr int WS_O = 0, WS_ML = 262144, WS_EP = WS_ML + 65536, WS_END = WS_EP + NGRP * 4352;
constexpr int AUX_NT = 2, AUX_SC1 = 16;
constexpr unsigned PCOL[16] = {PATC};
constexpr unsigned OOB = 0x80000000u, EMPTY = 0xFFFFFFFFu;

__device__ __forceinline__ unsigned cvtpk(float lo, float hi) {
  typedef __attribute__((ext_vector_type(2))) __bf16 bf16x2;
  f32x2 v = {lo, hi};
  return __builtin_bit_cast(unsigned, __builtin_convertvector(v, bf16x2));
}
template <int CTRL> __device__ __forceinline__ float dpp(float x) {
  return __builtin_bit_cast(float, __builtin_amdgcn_mov_dpp(__builtin_bit_cast(int, x), CTRL, 0xf, 0xf, true));
}
template <int I> __device__ __forceinline__ float rowb(float x) { return dpp<0x150 + I>(x); }
__device__ __forceinline__ float x16_sum(float x) {
  auto s = __builtin_amdgcn_permlane16_swap(__float_as_uint(x), __float_as_uint(x), false, false);
  return __uint_as_float(s[0]) + __uint_as_float(s[1]);
}
__device__ __forceinline__ float xh_max(float x) {
  auto s = __builtin_amdgcn_permlane16_swap(__float_as_uint(x), __float_as_uint(x), false, false);
  x = fmaxf(__uint_as_float(s[0]), __uint_as_float(s[1]));
  auto t = __builtin_amdgcn_permlane32_swap(__float_as_uint(x), __float_as_uint(x), false, false);
  return fmaxf(__uint_as_float(t[0]), __uint_as_float(t[1]));
}
__device__ __forceinline__ float max8(float x) { x = fmaxf(x, dpp<0xB1>(x)); x = fmaxf(x, dpp<0x4E>(x)); return fmaxf(x, dpp<0x141>(x)); }
__device__ __forceinline__ float sum8(float x) { x += dpp<0xB1>(x); x += dpp<0x4E>(x); return x + dpp<0x141>(x); }
__device__ __forceinline__ float sum16(float x) { x = sum8(x); return x + dpp<0x128>(x); }
__device__ __forceinline__ float lo16(unsigned x) { return __uint_as_float(x << 16); }
__device__ __forceinline__ float hi16(unsigned x) { return __uint_as_float(x & 0xFFFF0000u); }
__device__ __forceinline__ unsigned umax4(u32x4 v) { return max(max(v[0], v[1]), max(v[2], v[3])); }
#define RLX_AGENT __ATOMIC_RELAXED, __HIP_MEMORY_SCOPE_AGENT
#define RLX_WG __ATOMIC_RELAXED, __HIP_MEMORY_SCOPE_WORKGROUP
#define CFENCE() asm volatile("" ::: "memory")
#define SB() __builtin_amdgcn_sched_barrier(0)
#define WG_BARRIER() asm volatile("s_waitcnt lgkmcnt(0)\n\ts_barrier" ::: "memory")
#define PACE_TXT "1:\n\ts_memrealtime vcc\n\ts_waitcnt lgkmcnt(0)\n\ts_lshl_b32 vcc_lo, vcc_lo, 6\n\t" \
    "s_sub_i32 vcc_lo, vcc_lo, %0\n\ts_cmp_lt_i32 vcc_lo, 0\n\ts_cbranch_scc1 1b\n\ts_add_i32 %0, %0, %1"
#define PACE(t, d) asm volatile(PACE_TXT : "+s"(t) : "s"(d) : "vcc", "scc", "memory")
#define PACEC(t, d, thr, thr2) asm volatile(PACE_TXT "\n\ts_cmpk_ge_i32 vcc_lo, %3\n\ts_cbranch_scc1 3f\n\ts_cmpk_ge_i32 vcc_lo, %2\n\ts_cbranch_scc1 2f\n\t" \
    "s_waitcnt vmcnt(4)\n2:\n\ts_waitcnt vmcnt(5)\n3:\n\ts_waitcnt vmcnt(6)" : "+s"(t) : "s"(d), "i"(thr), "i"(thr2) : "vcc", "scc", "memory")
#define PACEE(t, d, thr, thr2) asm volatile(PACE_TXT "\n\ts_cmp_ge_i32 vcc_lo, %3\n\ts_cbranch_scc1 3f\n\ts_cmpk_ge_i32 vcc_lo, %2\n\ts_cbranch_scc1 2f\n\t" \
    "s_waitcnt vmcnt(4)\n2:\n\ts_waitcnt vmcnt(5)\n3:\n\ts_waitcnt vmcnt(6)" : "+s"(t) : "s"(d), "i"(thr), "s"(thr2) : "vcc", "scc", "memory")
#define ORD(i) (((i) & 3) == 0 ? 1 : ((i) & 3) == 1 ? 3 : ((i) & 3) == 2 ? 2 : 4)
#define THR(i) (LAGL * 16 * ORD(i))
#define THR2(i) (LAG2 * 64 + GR2 * 16 * ORD(i))
#define NOW64() ((unsigned)__builtin_amdgcn_s_memrealtime() << 6)
#define NAP(t) do { const unsigned t_ = (t); while ((int)(NOW64() - t_) < 0) __builtin_amdgcn_s_sleep(4); } while (0)

__global__ void __launch_bounds__(WAVES * 64)
tree_attn_decode(const unsigned short*, const unsigned short*, const unsigned short*, const unsigned short*, const unsigned short*,
                 const unsigned short*, unsigned short*, unsigned char*) {
  u64x8 args;
  unsigned long long tb64;
  asm volatile("s_load_dwordx16 %0, %2, 0x0\n\ts_memrealtime %1" : "=&s"(args), "=&s"(tb64) : "s"(__builtin_amdgcn_kernarg_segment_ptr()));
  SB();
  __shared__ __attribute__((aligned(16))) float lds[2048 + 1024 + 16];
  float* lds_o = lds + 2048;
  float* lds_m = lds_o + 1024;
  STAMP_DECL
  STAMP(0)
  const int bid = blockIdx.x, g = bid >> 3, s = bid & 7;
  const int tid = threadIdx.x, lane = tid & 63, wave = __builtin_amdgcn_readfirstlane(tid >> 6);
  const int r = lane & 15, h = lane >> 4;
  if (wave >= 4) {
    unsigned long long pc;
    unsigned j0, j1, j2, j3;
    asm volatile("s_getpc_b64 %0" : "=s"(pc));
    asm volatile("s_load_dword %0, %4, 0x0\n\ts_load_dword %1, %4, 0x40\n\ts_load_dword %2, %4, 0x80\n\ts_load_dword %3, %4, 0xc0\n\t"
                 "s_barrier\n\ts_waitcnt lgkmcnt(0)"
                 : "=&s"(j0), "=&s"(j1), "=&s"(j2), "=&s"(j3) : "s"((pc & ~255ull) + g * 256) : "memory");
  }
  const unsigned kvo = (r >> 2) * 256 + h * 64 + (r & 3) * 16, vvo = lane * 16;
  unsigned c0 = 0;
#pragma unroll
  for (int b = 0; b < 2; ++b) c0 ^= ((wave >> b) & 1) ? PCOL[6 + b] : 0u;
#pragma unroll
  for (int b = 0; b < 8; ++b) c0 ^= ((bid >> b) & 1) ? PCOL[8 + b] : 0u;
  const unsigned reg = ((c0 & 0x7FF) << 10) | (g << 21);
#define LOFF(i) (((((i) & 1) ? PCOL[0] : 0u) ^ (((i) & 2) ? PCOL[1] : 0u) ^ (((i) & 4) ? PCOL[2] : 0u)) << 10)
#define BOFF(b) (reg ^ (((((b) & 1) ? PCOL[3] : 0u) ^ (((b) & 2) ? PCOL[4] : 0u) ^ (((b) & 4) ? PCOL[5] : 0u)) << 10))
  const unsigned t0 = ((wave * 8 + (c0 >> 11)) * TOTAL >> 6) + HEAD * 64, dt = TOTAL / 2;
  const unsigned qo = (g * 2 + ((r >> 2) & 1)) * 256 + (4 * h + (r & 3)) * 16;
  lf32* mp = (lf32*)(lds_m + (lane & 4) + (wave & 3) + (wave & 4) * 2);
  *mp = __uint_as_float(EMPTY);
  asm volatile("" :: "s"(t0), "s"(reg), "v"(kvo), "v"(qo) : "memory");
  SB();
  asm volatile("s_waitcnt lgkmcnt(0)" : "+s"(args), "+s"(tb64));
  const gu16* q = (const gu16*)args[0];
  gu8* ws = (gu8*)args[7];
  unsigned tb = (unsigned)tb64 << 6;
  if (INJS) tb += ((((s + 8 * (tb >> 18)) * 2654435761u) >> 16) % (unsigned)(INJS + 1)) * 64;
  if (wave >= 4) {
    const __amdgpu_buffer_rsrc_t rw = __builtin_amdgcn_make_buffer_rsrc((void*)ws, 0, WS_END, 0x00020000);
    NAP(tb + 250 * 64);
    if (SPFV) { unsigned j0, j1; asm volatile("s_load_dword %0, %2, 0x0\n\ts_load_dword %1, %3, 0x0\n\ts_waitcnt lgkmcnt(0)" : "=&s"(j0), "=&s"(j1) : "s"(args[2] + reg), "s"(args[7] + bid * 1024) : "memory"); }
    if (SPFV == 2) { const unsigned jv = *(const volatile GAS unsigned*)(args[2] + reg); asm volatile("" :: "v"(jv)); }
    const unsigned tag = __hip_atomic_load((gu32*)(ws + WS_EP + g * 4352), RLX_AGENT) + 1u;
    __builtin_amdgcn_raw_buffer_store_b128(u32x4{EMPTY, EMPTY, EMPTY, EMPTY}, rw, lane * 16, WS_O + bid * 1024, AUX_SC1);
    float st = 0.f;
    u32x2 vv = {0u, 0u};
    if (s == 0) {
      const u32x2 kk = *(const GAS u32x2*)((const gu16*)args[3] + g * HD + 4 * (lane & 31));
      const u32x2 qq = *(const GAS u32x2*)(q + g * 2 * HD + 4 * lane);
      vv = *(const GAS u32x2*)((const gu16*)args[4] + g * HD + 4 * (lane & 31));
      const float bias = (1.0f - lo16(((const gu16*)args[5])[g / HKV])) * -1000000000.0f;
      st = (lo16(kk[0]) * lo16(qq[0]) + hi16(kk[0]) * hi16(qq[0])) + (lo16(kk[1]) * lo16(qq[1]) + hi16(kk[1]) * hi16(qq[1]));
      st = (x16_sum(sum16(st)) * SCALE + bias) * LOG2E;
    }
    asm volatile("s_waitcnt vmcnt(0)" ::: "memory");
    f32x4 w4;
    do {
      __builtin_amdgcn_s_sleep(8);
      CFENCE();
      w4 = *reinterpret_cast<const f32x4*>(lds_m + (lane >> 5) * 4);
    } while (__builtin_amdgcn_uicmp(umax4(__builtin_bit_cast(u32x4, w4)), EMPTY, 32));
    CFENCE();
    STAMP(1)
    const float M = fmaxf(fmaxf(w4[0], w4[1]), fmaxf(w4[2], w4[3]));
#pragma unroll
    for (int c = 0; c < 4; ++c) w4[c] = __builtin_amdgcn_exp2f(w4[c] - M);
    float L = 0.f;
#pragma unroll 1
    for (int k = 0; k < 8; ++k) {
      const f32x4 x = *reinterpret_cast<const f32x4*>(lds + (lane >> 5) * 1024 + k * 128 + (lane & 31) * 4);
      L += (__builtin_amdgcn_exp2f(fmaf(x[0], C2, -M)) + __builtin_amdgcn_exp2f(fmaf(x[1], C2, -M))) +
           (__builtin_amdgcn_exp2f(fmaf(x[2], C2, -M)) + __builtin_amdgcn_exp2f(fmaf(x[3], C2, -M)));
    }
    L = x16_sum(sum16(L));
#define REC(i) (*reinterpret_cast<const f32x4*>(lds_o + (i) * 2 * HD + 4 * lane))
    if (s != 0) {
      const u32x4 gm = {(lane & 1) ? tb : __float_as_uint(M), tag, __float_as_uint(L), tag};
      __builtin_amdgcn_raw_buffer_store_b128(gm, rw, (lane & 24) == 0 ? (lane >> 5) * 128 + (lane & 7) * 16 : OOB, WS_ML + bid * 256, AUX_SC1);
      STAMP(3)
      WG_BARRIER();
      STAMP(4)
      u32x4 go = __builtin_bit_cast(u32x4, (w4[0] * REC(0) + w4[1] * REC(1)) + (w4[2] * REC(2) + w4[3] * REC(3)));
#pragma unroll
      for (int c = 0; c < 4; ++c) go[c] = min(go[c], EMPTY - 1u);
      __builtin_amdgcn_raw_buffer_store_b128(go, rw, lane * 16, WS_O + bid * 1024, AUX_SC1);
    } else {
      NAP(NOW64() + 150 * 64);
      u32x4 a;
      for (;;) {
        CFENCE();
        a = __builtin_amdgcn_raw_buffer_load_b128(rw, (lane & 7) * 256 + (lane >> 5) * 128 + (lane & 8) * 2, WS_ML + bid * 256, AUX_SC1);
        if (((__builtin_amdgcn_uicmp(a[1], tag, 33) | __builtin_amdgcn_uicmp(a[3], tag, 33)) & 0xFEFEFEFEFEFEFEFEull) == 0) break;
        __builtin_amdgcn_s_sleep(16);
      }
      if (lane == 0) __hip_atomic_store((gu32*)(ws + WS_EP + g * 4352), tag, RLX_AGENT);
      const float mk = (lane & 7) == 0 ? M : __uint_as_float(a[0]), lk = (lane & 7) == 0 ? L : __uint_as_float(a[2]);
      const float Mx = max8(fmaxf(mk, st));
      const float wk = __builtin_amdgcn_exp2f(mk - Mx), wt = __builtin_amdgcn_exp2f(st - Mx);
      const float inv = 1.0f / (sum8(wk * lk) + wt), ck = wk * inv;
      w4 *= rowb<0>(ck);
      const float c1 = rowb<1>(ck), c2 = rowb<2>(ck), c3 = rowb<3>(ck), c4 = rowb<4>(ck), c5 = rowb<5>(ck), c6 = rowb<6>(ck), c7 = rowb<7>(ck);
      f32x4 o = rowb<0>(wt * inv) * f32x4{lo16(vv[0]), hi16(vv[0]), lo16(vv[1]), hi16(vv[1])};
      const float skew = max8((lane & 7) == 0 ? 0.f : (float)(int)(a[0] - tb));
      const unsigned te = tb + (HEAD + TOTAL + PDELAY) * 64 + __builtin_amdgcn_readlane((int)skew, 8);
      gu64* op = (gu64*)((gu16*)args[6] + g * 2 * HD + 4 * lane);
      STAMP(3)
      WG_BARRIER();
      STAMP(4)
      unsigned tp = NOW64() + POWN * 64;
      o += (w4[0] * REC(0) + w4[1] * REC(1)) + (w4[2] * REC(2) + w4[3] * REC(3));
      if ((int)(te - tp) > 0) tp = te;
      PACE(tp, dt);
      u32x4 po[NSPLIT];
      unsigned mx;
      do {
        CFENCE();
#pragma unroll
        for (int k = 1; k < NSPLIT; ++k) po[k] = __builtin_amdgcn_raw_buffer_load_b128(rw, lane * 16 + (k & 3) * 1024, WS_O + bid * 1024 + (k >> 2) * 4096, AUX_SC1);
        mx = umax4(po[1]);
#pragma unroll
        for (int k = 2; k < NSPLIT; ++k) mx = max(mx, umax4(po[k]));
      } while (__builtin_amdgcn_uicmp(mx, EMPTY, 32));
#define PO(k) __builtin_bit_cast(f32x4, po[k])
      o = ((o + c1 * PO(1)) + (c2 * PO(2) + c3 * PO(3))) + ((c4 * PO(4) + c5 * PO(5)) + (c6 * PO(6) + c7 * PO(7)));
      __hip_atomic_store(op, (unsigned long long)cvtpk(o[0], o[1]) | ((unsigned long long)cvtpk(o[2], o[3]) << 32), RLX_AGENT);
      if (DPROBE) { asm volatile("s_waitcnt vmcnt(0)" ::: "memory"); const unsigned until = tb + 3000 * 64 + 5 * (NOW64() - tb - 1800 * 64); while ((int)(NOW64() - until) < 0) __builtin_amdgcn_s_sleep(1); }
    }
    STAMP(5)
  } else {
    if (INJC | INJR | INJS) { const unsigned dl = 25 + INJC + (INJR ? (((bid + (tb >> 18)) * 2654435761u) >> 16) % (unsigned)(INJR + 1) : 0u); while ((int)(NOW64() - tb) < (int)dl * 64) {} }
    const __amdgpu_buffer_rsrc_t rk = __builtin_amdgcn_make_buffer_rsrc((void*)args[1], 0, NGRP * CTX * HD * 2, 0x00020000);
    const __amdgpu_buffer_rsrc_t rv = __builtin_amdgcn_make_buffer_rsrc((void*)args[2], 0, NGRP * CTX * HD * 2, 0x00020000);
    unsigned tn = tb + t0 + NSKIP * dt;
    u32x4 buf[8], qf;
#pragma unroll
    for (int i = 0; i < 8; ++i) {
      if (i >= 5) PACEC(tn, dt, THR(i), THR2(i)); else if (i >= NSKIP) PACE(tn, dt);
      buf[i] = __builtin_amdgcn_raw_buffer_load_b128(rk, kvo, reg ^ LOFF(i), AUX_NT);
      SB();
      if (i == 0) { qf = *(const GAS u32x4*)((const gu8*)q + qo); SB(); }
      if (i == NSKIP - 1) __builtin_amdgcn_s_barrier();
    }
    const int jj = (r & 3) + 4 * (r >> 3);
    unsigned msk[4];
#pragma unroll
    for (int c = 0; c < 4; ++c) msk[c] = (c == (jj >> 1)) ? ((jj & 1) ? 0xFFFF0000u : 0x0000FFFFu) : 0u;
    asm volatile("" : "+v"(msk[0]), "+v"(msk[1]), "+v"(msk[2]), "+v"(msk[3]), "+v"(mp));
    float* sp = lds + (((lane >> 2) & 1) * 4 + wave) * 256 + h * 8;
    float m = -INFINITY;
#pragma unroll 1
    for (int b = 1; b <= 8; ++b) {
      const __amdgpu_buffer_rsrc_t rs = __builtin_amdgcn_make_buffer_rsrc((void*)(b < 8 ? args[1] : args[2]), 0, NGRP * CTX * HD * 2, 0x00020000);
      const unsigned vo = b < 8 ? kvo : vvo, bo = BOFF(b);
      float S[8];
#pragma unroll
      for (int i = 0; i < 8; ++i) {
        const f32x4 z = {0.f, 0.f, 0.f, 0.f};
        const f32x4 d = __builtin_amdgcn_mfma_f32_16x16x32_bf16(__builtin_bit_cast(bf16x8, buf[i]), __builtin_bit_cast(bf16x8, qf), z, 0, 0, 0);
        S[i] = (dpp<0x00>(d[0]) + dpp<0x55>(d[1])) + (dpp<0xAA>(d[2]) + dpp<0xFF>(d[3]));
        m = fmaxf(m, S[i]);
        asm volatile("" : "+v"(m));
        if ((i & 3) == 3) *reinterpret_cast<f32x4*>(sp + (b - 1) * 32 + (i & 4)) = f32x4{S[i - 3], S[i - 2], S[i - 1], S[i]};
        SB();
        PACEC(tn, dt, THR(i), THR2(i));
        buf[i] = __builtin_amdgcn_raw_buffer_load_b128(rs, vo, bo ^ LOFF(i), AUX_NT);
        SB();
      }
    }
    f32x4 s0 = *reinterpret_cast<const f32x4*>(sp), s1 = *reinterpret_cast<const f32x4*>(sp + 4);
    const float mc = xh_max(m) * C2;
    CFENCE();
    *mp = __uint_as_float(min(__float_as_uint(mc), EMPTY - 1u));
    f32x4 acc = {0.f, 0.f, 0.f, 0.f};
#define PV(sc, i) do { const float p_ = __builtin_amdgcn_exp2f(fmaf(sc, C2, -mc)); const unsigned pp_ = cvtpk(p_, p_); \
      const u32x4 a_ = {pp_ & msk[0], pp_ & msk[1], pp_ & msk[2], pp_ & msk[3]}; \
      acc = __builtin_amdgcn_mfma_f32_16x16x32_bf16(__builtin_bit_cast(bf16x8, a_), __builtin_bit_cast(bf16x8, buf[i]), acc, 0, 0, 0); SB(); } while (0)
#pragma unroll 1
    for (int b = 1; b < 8; ++b) {
      const unsigned bo = BOFF(b);
      const int te = b == 7 ? 0 : (LAG2 + GR2) * 64;
#pragma unroll
      for (int i = 0; i < 8; ++i) {
        PV(i < 4 ? s0[i & 3] : s1[i & 3], i);
        if (i == 3) s0 = *reinterpret_cast<const f32x4*>(sp + b * 32);
        if (i == 7) s1 = *reinterpret_cast<const f32x4*>(sp + b * 32 + 4);
        if (i >= 8 - NFREE) PACEE(tn, dt, THR(i), te); else PACEC(tn, dt, THR(i), THR2(i));
        buf[i] = __builtin_amdgcn_raw_buffer_load_b128(rv, vvo, bo ^ LOFF(i), AUX_NT);
        SB();
      }
    }
#pragma unroll
    for (int i = 0; i < 8; ++i) PV(i < 4 ? s0[i & 3] : s1[i & 3], i);
    *reinterpret_cast<f32x4*>(lds_o + wave * 2 * HD + (h & 1) * HD + 8 * r + 4 * (h >> 1)) = acc;
    WG_BARRIER();
  }
  STAMP_OUT
}

extern "C" void kernel_launch(void* const* d_in, const int* in_sizes, int n_in, void* d_out, int out_size, void* d_ws, size_t ws_size,
                              hipStream_t stream) {
  tree_attn_decode<<<NGRP * NSPLIT, WAVES * 64, 0, stream>>>((const unsigned short*)d_in[0], (const unsigned short*)d_in[1], (const unsigned short*)d_in[2],
                                                             (const unsigned short*)d_in[3], (const unsigned short*)d_in[4], (const unsigned short*)d_in[5],
                                                             (unsigned short*)d_out, (unsigned char*)d_ws);
}
```

```cpp
#include <hip/hip_runtime.h>
#include <hip/hip_bf16.h>
#include <stdint.h>

typedef __attribute__((ext_vector_type(8))) __bf16 bf16x8;
typedef __attribute__((ext_vector_type(4))) float f32x4;
typedef __attribute__((ext_vector_type(2))) float f32x2;
typedef __attribute__((ext_vector_type(4))) unsigned u32x4;
typedef __attribute__((ext_vector_type(2))) unsigned u32x2;
typedef __attribute__((ext_vector_type(8))) unsigned long long u64x8;
#define GAS __attribute__((address_space(1)))
typedef GAS unsigned char gu8;
typedef GAS unsigned short gu16;
typedef GAS unsigned gu32;
typedef GAS unsigned long long gu64;
typedef __attribute__((address_space(3))) float lf32;

#define TOTAL 1706
#define HEAD 0
#define PDELAY 90
#define POWN 15
#define LAGL 40
#define LAG2 40
#define GR2 40
#define NSKIP 2
#define NFREE 4
#define DPROBE 0
#define INJC 0
#define INJR 0
#define INJS 0
#define PATC 0x40, 0x66f, 0x6d4, 0x4e5, 0x200, 0xe1, 0x2a5, 0x28, 0x2948, 0x1029, 0x33cd, 0x829, 0xc9d, 0x46c, 0x805, 0x78f
#define SPFV 1
#define STAMP(i)
#define STAMP_DECL
#define STAMP_OUT

constexpr int NB = 2, HQ = 32, HKV = 16, CTX = 8192, HD = 128;
constexpr int NGRP = NB * HKV;
constexpr int NSPLIT = 8, WAVES = 5;
constexpr float SCALE = 0.088388347648318440f, LOG2E = 1.4426950408889634f;
constexpr float C2 = SCALE * LOG2E;
constexpr int WS_O = 0, WS_ML = 262144, WS_EP = WS_ML + 65536, WS_END = WS_EP + NGRP * 4352;
constexpr int AUX_NT = 2, AUX_SC1 = 16;
constexpr unsigned PCOL[16] = {PATC};
constexpr unsigned OOB = 0x80000000u, EMPTY = 0xFFFFFFFFu;

__device__ __forceinline__ unsigned cvtpk(float lo, float hi) {
  typedef __attribute__((ext_vector_type(2))) __bf16 bf16x2;
  f32x2 v = {lo, hi};
  return __builtin_bit_cast(unsigned, __builtin_convertvector(v, bf16x2));
}
template <int CTRL> __device__ __forceinline__ float dpp(float x) {
  return __builtin_bit_cast(float, __builtin_amdgcn_mov_dpp(__builtin_bit_cast(int, x), CTRL, 0xf, 0xf, true));
}
template <int I> __device__ __forceinline__ float rowb(float x) { return dpp<0x150 + I>(x); }
__device__ __forceinline__ float x16_sum(float x) {
  auto s = __builtin_amdgcn_permlane16_swap(__float_as_uint(x), __float_as_uint(x), false, false);
  return __uint_as_float(s[0]) + __uint_as_float(s[1]);
}
__device__ __forceinline__ float xh_max(float x) {
  auto s = __builtin_amdgcn_permlane16_swap(__float_as_uint(x), __float_as_uint(x), false, false);
  x = fmaxf(__uint_as_float(s[0]), __uint_as_float(s[1]));
  auto t = __builtin_amdgcn_permlane32_swap(__float_as_uint(x), __float_as_uint(x), false, false);
  return fmaxf(__uint_as_float(t[0]), __uint_as_float(t[1]));
}
__device__ __forceinline__ float max8(float x) { x = fmaxf(x, dpp<0xB1>(x)); x = fmaxf(x, dpp<0x4E>(x)); return fmaxf(x, dpp<0x141>(x)); }
__device__ __forceinline__ float sum8(float x) { x += dpp<0xB1>(x); x += dpp<0x4E>(x); return x + dpp<0x141>(x); }
__device__ __forceinline__ float sum16(float x) { x = sum8(x); return x + dpp<0x128>(x); }
__device__ __forceinline__ float lo16(unsigned x) { return __uint_as_float(x << 16); }
__device__ __forceinline__ float hi16(unsigned x) { return __uint_as_float(x & 0xFFFF0000u); }
__device__ __forceinline__ unsigned umax4(u32x4 v) { return max(max(v[0], v[1]), max(v[2], v[3])); }
#define RLX_AGENT __ATOMIC_RELAXED, __HIP_MEMORY_SCOPE_AGENT
#define RLX_WG __ATOMIC_RELAXED, __HIP_MEMORY_SCOPE_WORKGROUP
#define CFENCE() asm volatile("" ::: "memory")
#define SB() __builtin_amdgcn_sched_barrier(0)
#define WG_BARRIER() asm volatile("s_waitcnt lgkmcnt(0)\n\ts_barrier" ::: "memory")
#define PACE_TXT "1:\n\ts_memrealtime vcc\n\ts_waitcnt lgkmcnt(0)\n\ts_lshl_b32 vcc_lo, vcc_lo, 6\n\t" \
    "s_sub_i32 vcc_lo, vcc_lo, %0\n\ts_cmp_lt_i32 vcc_lo, 0\n\ts_cbranch_scc1 1b\n\ts_add_i32 %0, %0, %1"
#define PACE(t, d) asm volatile(PACE_TXT : "+s"(t) : "s"(d) : "vcc", "scc", "memory")
#define PACEC(t, d, thr, thr2) asm volatile(PACE_TXT "\n\ts_cmpk_ge_i32 vcc_lo, %3\n\ts_cbranch_scc1 3f\n\ts_cmpk_ge_i32 vcc_lo, %2\n\ts_cbranch_scc1 2f\n\t" \
    "s_waitcnt vmcnt(4)\n2:\n\ts_waitcnt vmcnt(5)\n3:\n\ts_waitcnt vmcnt(6)" : "+s"(t) : "s"(d), "i"(thr), "i"(thr2) : "vcc", "scc", "memory")
#define PACEE(t, d, thr, thr2) asm volatile(PACE_TXT "\n\ts_cmp_ge_i32 vcc_lo, %3\n\ts_cbranch_scc1 3f\n\ts_cmpk_ge_i32 vcc_lo, %2\n\ts_cbranch_scc1 2f\n\t" \
    "s_waitcnt vmcnt(4)\n2:\n\ts_waitcnt vmcnt(5)\n3:\n\ts_waitcnt vmcnt(6)" : "+s"(t) : "s"(d), "i"(thr), "s"(thr2) : "vcc", "scc", "memory")
#define ORD(i) (((i) & 3) == 0 ? 1 : ((i) & 3) == 1 ? 3 : ((i) & 3) == 2 ? 2 : 4)
#define THR(i) (LAGL * 16 * ORD(i))
#define THR2(i) (LAG2 * 64 + GR2 * 16 * ORD(i))
#define NOW64() ((unsigned)__builtin_amdgcn_s_memrealtime() << 6)
#define NAP(t) do { const unsigned t_ = (t); while ((int)(NOW64() - t_) < 0) __builtin_amdgcn_s_sleep(4); } while (0)

__global__ void __launch_bounds__(WAVES * 64)
tree_attn_decode(const unsigned short*, const unsigned short*, const unsigned short*, const unsigned short*, const unsigned short*,
                 const unsigned short*, unsigned short*, unsigned char*) {
  u64x8 args;
  unsigned long long tb64;
  asm volatile("s_load_dwordx16 %0, %2, 0x0\n\ts_memrealtime %1" : "=&s"(args), "=&s"(tb64) : "s"(__builtin_amdgcn_kernarg_segment_ptr()));
  SB();
  __shared__ __attribute__((aligned(16))) float lds[2048 + 1024 + 16 + 16];
  float* lds_o = lds + 2048;
  float* lds_m = lds_o + 1024;
  STAMP_DECL
  STAMP(0)
  const int bid = blockIdx.x, g = bid >> 3, s = bid & 7;
  const int tid = threadIdx.x, lane = tid & 63, wave = __builtin_amdgcn_readfirstlane(tid >> 6);
  const int r = lane & 15, h = lane >> 4;
  if (wave >= 4) {
    unsigned long long pc;
    unsigned j0, j1, j2, j3;
    asm volatile("s_getpc_b64 %0" : "=s"(pc));
    asm volatile("s_load_dword %0, %4, 0x0\n\ts_load_dword %1, %4, 0x40\n\ts_load_dword %2, %4, 0x80\n\ts_load_dword %3, %4, 0xc0\n\t"
                 "s_barrier\n\ts_waitcnt lgkmcnt(0)"
                 : "=&s"(j0), "=&s"(j1), "=&s"(j2), "=&s"(j3) : "s"((pc & ~255ull) + g * 256) : "memory");
  }
  const unsigned kvo = (r >> 2) * 256 + h * 64 + (r & 3) * 16, vvo = lane * 16;
  unsigned c0 = 0;
#pragma unroll
  for (int b = 0; b < 2; ++b) c0 ^= ((wave >> b) & 1) ? PCOL[6 + b] : 0u;
#pragma unroll
  for (int b = 0; b < 8; ++b) c0 ^= ((bid >> b) & 1) ? PCOL[8 + b] : 0u;
  const unsigned reg = ((c0 & 0x7FF) << 10) | (g << 21);
#define LOFF(i) (((((i) & 1) ? PCOL[0] : 0u) ^ (((i) & 2) ? PCOL[1] : 0u) ^ (((i) & 4) ? PCOL[2] : 0u)) << 10)
#define BOFF(b) (reg ^ (((((b) & 1) ? PCOL[3] : 0u) ^ (((b) & 2) ? PCOL[4] : 0u) ^ (((b) & 4) ? PCOL[5] : 0u)) << 10))
  const unsigned t0 = ((wave * 8 + (c0 >> 11)) * TOTAL >> 6) + HEAD * 64, dt = TOTAL / 2;
  const unsigned qo = (g * 2 + ((r >> 2) & 1)) * 256 + (4 * h + (r & 3)) * 16;
  lf32* mp = (lf32*)(lds_m + (lane & 4) + (wave & 3) + (wave & 4) * 2);
  *mp = __uint_as_float(EMPTY);
  asm volatile("" :: "s"(t0), "s"(reg), "v"(kvo), "v"(qo) : "memory");
  SB();
  asm volatile("s_waitcnt lgkmcnt(0)" : "+s"(args), "+s"(tb64));
  const gu16* q = (const gu16*)args[0];
  gu8* ws = (gu8*)args[7];
  unsigned tb = (unsigned)tb64 << 6;
  if (INJS) tb += ((((s + 8 * (tb >> 18)) * 2654435761u) >> 16) % (unsigned)(INJS + 1)) * 64;
  if (wave >= 4) {
    const __amdgpu_buffer_rsrc_t rw = __builtin_amdgcn_make_buffer_rsrc((void*)ws, 0, WS_END, 0x00020000);
    NAP(tb + 250 * 64);
    if (SPFV) { unsigned j0, j1; asm volatile("s_load_dword %0, %2, 0x0\n\ts_load_dword %1, %3, 0x0\n\ts_waitcnt lgkmcnt(0)" : "=&s"(j0), "=&s"(j1) : "s"(args[2] + reg), "s"(args[7] + bid * 1024) : "memory"); }
    if (SPFV == 2) { const unsigned jv = *(const volatile GAS unsigned*)(args[2] + reg); asm volatile("" :: "v"(jv)); }
    const unsigned tag = __hip_atomic_load((gu32*)(ws + WS_EP + g * 4352), RLX_AGENT) + 1u;
    __builtin_amdgcn_raw_buffer_store_b128(u32x4{EMPTY, EMPTY, EMPTY, EMPTY}, rw, lane * 16, WS_O + bid * 1024, AUX_SC1);
    float st = 0.f;
    u32x2 vv = {0u, 0u};
    if (s == 0) {
      const u32x2 kk = *(const GAS u32x2*)((const gu16*)args[3] + g * HD + 4 * (lane & 31));
      const u32x2 qq = *(const GAS u32x2*)(q + g * 2 * HD + 4 * lane);
      vv = *(const GAS u32x2*)((const gu16*)args[4] + g * HD + 4 * (lane & 31));
      const float bias = (1.0f - lo16(((const gu16*)args[5])[g / HKV])) * -1000000000.0f;
      st = (lo16(kk[0]) * lo16(qq[0]) + hi16(kk[0]) * hi16(qq[0])) + (lo16(kk[1]) * lo16(qq[1]) + hi16(kk[1]) * hi16(qq[1]));
      st = (x16_sum(sum16(st)) * SCALE + bias) * LOG2E;
    }
    asm volatile("s_waitcnt vmcnt(0)" ::: "memory");
    f32x4 w4;
    do {
      __builtin_amdgcn_s_sleep(8);
      CFENCE();
      w4 = *reinterpret_cast<const f32x4*>(lds_m + (lane >> 5) * 4);
    } while (__builtin_amdgcn_uicmp(umax4(__builtin_bit_cast(u32x4, w4)), EMPTY, 32));
    CFENCE();
    STAMP(1)
    const float M = fmaxf(fmaxf(w4[0], w4[1]), fmaxf(w4[2], w4[3]));
#pragma unroll
    for (int c = 0; c < 4; ++c) w4[c] = __builtin_amdgcn_exp2f(w4[c] - M);
    float L = 0.f;
#pragma unroll 1
    for (int k = 0; k < 8; ++k) {
      const f32x4 x = *reinterpret_cast<const f32x4*>(lds + (lane >> 5) * 1024 + k * 128 + (lane & 31) * 4);
      L += (__builtin_amdgcn_exp2f(fmaf(x[0], C2, -M)) + __builtin_amdgcn_exp2f(fmaf(x[1], C2, -M))) +
           (__builtin_amdgcn_exp2f(fmaf(x[2], C2, -M)) + __builtin_amdgcn_exp2f(fmaf(x[3], C2, -M)));
    }
    L = x16_sum(sum16(L));
#define REC(i) (*reinterpret_cast<const f32x4*>(lds_o + (i) * 2 * HD + 4 * lane))
    if (s != 0) {
      const u32x4 gm = {(lane & 1) ? tb : __float_as_uint(M), tag, __float_as_uint(L), tag};
      __builtin_amdgcn_raw_buffer_store_b128(gm, rw, (lane & 24) == 0 ? (lane >> 5) * 128 + (lane & 7) * 16 : OOB, WS_ML + bid * 256, AUX_SC1);
      STAMP(3)
      WG_BARRIER();
      STAMP(4)
      u32x4 go = __builtin_bit_cast(u32x4, (w4[0] * REC(0) + w4[1] * REC(1)) + (w4[2] * REC(2) + w4[3] * REC(3)));
#pragma unroll
      for (int c = 0; c < 4; ++c) go[c] = min(go[c], EMPTY - 1u);
      __builtin_amdgcn_raw_buffer_store_b128(go, rw, lane * 16, WS_O + bid * 1024, AUX_SC1);
    } else {
      NAP(NOW64() + 150 * 64);
      u32x4 a;
      for (;;) {
        CFENCE();
        a = __builtin_amdgcn_raw_buffer_load_b128(rw, (lane & 7) * 256 + (lane >> 5) * 128 + (lane & 8) * 2, WS_ML + bid * 256, AUX_SC1);
        if (((__builtin_amdgcn_uicmp(a[1], tag, 33) | __builtin_amdgcn_uicmp(a[3], tag, 33)) & 0xFEFEFEFEFEFEFEFEull) == 0) break;
        __builtin_amdgcn_s_sleep(16);
      }
      if (lane == 0) __hip_atomic_store((gu32*)(ws + WS_EP + g * 4352), tag, RLX_AGENT);
      const float mk = (lane & 7) == 0 ? M : __uint_as_float(a[0]), lk = (lane & 7) == 0 ? L : __uint_as_float(a[2]);
      const float Mx = max8(fmaxf(mk, st));
      const float wk = __builtin_amdgcn_exp2f(mk - Mx), wt = __builtin_amdgcn_exp2f(st - Mx);
      const float inv = 1.0f / (sum8(wk * lk) + wt), ck = wk * inv;
      w4 *= rowb<0>(ck);
      const float c1 = rowb<1>(ck), c2 = rowb<2>(ck), c3 = rowb<3>(ck), c4 = rowb<4>(ck), c5 = rowb<5>(ck), c6 = rowb<6>(ck), c7 = rowb<7>(ck);
      f32x4 o = rowb<0>(wt * inv) * f32x4{lo16(vv[0]), hi16(vv[0]), lo16(vv[1]), hi16(vv[1])};
      const float skew = max8((lane & 7) == 0 ? 0.f : (float)(int)(a[0] - tb));
      const unsigned te = tb + (HEAD + TOTAL + PDELAY) * 64 + __builtin_amdgcn_readlane((int)skew, 8);
      gu64* op = (gu64*)((gu16*)args[6] + g * 2 * HD + 4 * lane);
      STAMP(3)
      WG_BARRIER();
      STAMP(4)
      unsigned tp = NOW64() + POWN * 64;
      o += (w4[0] * REC(0) + w4[1] * REC(1)) + (w4[2] * REC(2) + w4[3] * REC(3));
      if ((int)(te - tp) > 0) tp = te;
      PACE(tp, dt);
      u32x4 po[NSPLIT];
      unsigned mx;
      do {
        CFENCE();
#pragma unroll
        for (int k = 1; k < NSPLIT; ++k) po[k] = __builtin_amdgcn_raw_buffer_load_b128(rw, lane * 16 + (k & 3) * 1024, WS_O + bid * 1024 + (k >> 2) * 4096, AUX_SC1);
        mx = umax4(po[1]);
#pragma unroll
        for (int k = 2; k < NSPLIT; ++k) mx = max(mx, umax4(po[k]));
      } while (__builtin_amdgcn_uicmp(mx, EMPTY, 32));
#define PO(k) __builtin_bit_cast(f32x4, po[k])
      o = ((o + c1 * PO(1)) + (c2 * PO(2) + c3 * PO(3))) + ((c4 * PO(4) + c5 * PO(5)) + (c6 * PO(6) + c7 * PO(7)));
      __hip_atomic_store(op, (unsigned long long)cvtpk(o[0], o[1]) | ((unsigned long long)cvtpk(o[2], o[3]) << 32), RLX_AGENT);
      if (DPROBE) { asm volatile("s_waitcnt vmcnt(0)" ::: "memory"); const unsigned until = tb + 3000 * 64 + 5 * (NOW64() - tb - 1800 * 64); while ((int)(NOW64() - until) < 0) __builtin_amdgcn_s_sleep(1); }
    }
    {
      const unsigned tend = NOW64();
      NAP(tb + 2600 * 64);
      gu32* dg = (gu32*)(ws + (1 << 20));
      if (lane < 16) __hip_atomic_store(dg + bid * 32 + lane, ((unsigned*)lds)[3088 + lane], RLX_AGENT);
      if (lane == 16) __hip_atomic_store(dg + bid * 32 + 16, tb, RLX_AGENT);
      if (lane == 17) __hip_atomic_store(dg + bid * 32 + 17, tend, RLX_AGENT);
      if (bid == 0) {
        NAP(tb + 3200 * 64);
        float sm = 0.f, mx = -1e9f;
        for (int k = 0; k < 64; ++k) { const float v = (float)(int)__hip_atomic_load(dg + ((lane >> 4) * 64 + k) * 32 + (lane & 15), RLX_AGENT); sm += v; mx = fmaxf(mx, v); }
        sm = x16_sum(sm); { auto t = __builtin_amdgcn_permlane32_swap(__float_as_uint(sm), __float_as_uint(sm), false, false); sm = __uint_as_float(t[0]) + __uint_as_float(t[1]); }
        mx = xh_max(mx);
        float st_ = 0.f, l15 = 0.f, en = -1e9f;
        for (int k = 0; k < 32; ++k) { const int bb = k * 8 + (lane & 7);
          st_ += (float)((int)(__hip_atomic_load(dg + bb * 32 + 16, RLX_AGENT) - tb) >> 6) * (1.f / 32); l15 += (float)(int)__hip_atomic_load(dg + bb * 32 + 15, RLX_AGENT) * (1.f / 32);
          en = fmaxf(en, (float)((int)(__hip_atomic_load(dg + bb * 32 + 17, RLX_AGENT) - tb) >> 6)); }
        unsigned long long pk[10];
        for (int w = 0; w < 10; ++w) pk[w] = 0;
#pragma unroll
        for (int k = 0; k < 16; ++k) { pk[k >> 3] |= (unsigned long long)(min(__builtin_amdgcn_readlane((int)(sm * (1.f / 256)), k), 255) & 255) << (8 * (7 - (k & 7)));
                                       pk[2 + (k >> 3)] |= (unsigned long long)(min(__builtin_amdgcn_readlane((int)mx, k), 255) & 255) << (8 * (7 - (k & 7))); }
#pragma unroll
        for (int k = 0; k < 8; ++k) { pk[4] |= (unsigned long long)((__builtin_amdgcn_readlane((int)st_, k) + 64) & 255) << (8 * (7 - k)); pk[5] |= (unsigned long long)(min(__builtin_amdgcn_readlane((int)l15, k), 255) & 255) << (8 * (7 - k));
                                      pk[6] |= (unsigned long long)(min(__builtin_amdgcn_readlane((int)en, k) - 1780, 255) & 255) << (8 * (7 - k)); }
        if (lane == 0 && ((pk[0] >> 56) >= 0x30 && (pk[2] >> 56) * 10 <= (pk[0] >> 56) * 22)) {
          auto msg = __ockl_fprintf_stderr_begin();
          msg = __ockl_fprintf_append_string_n(msg, "D %016lx%016lx %016lx%016lx %016lx %016lx %016lx\n", 50, 0);
          __ockl_fprintf_append_args(msg, 7, pk[0], pk[1], pk[2], pk[3], pk[4], pk[5], pk[6], 1);
          if (((tb >> 8) * 2654435761u >> 16) % 300 == 999) ((gu16*)args[6])[0] = 0x7000;
        }
      }
    }
  } else {
    if (INJC | INJR | INJS) { const unsigned dl = 25 + INJC + (INJR ? (((bid + (tb >> 18)) * 2654435761u) >> 16) % (unsigned)(INJR + 1) : 0u); while ((int)(NOW64() - tb) < (int)dl * 64) {} }
    const __amdgpu_buffer_rsrc_t rk = __builtin_amdgcn_make_buffer_rsrc((void*)args[1], 0, NGRP * CTX * HD * 2, 0x00020000);
    const __amdgpu_buffer_rsrc_t rv = __builtin_amdgcn_make_buffer_rsrc((void*)args[2], 0, NGRP * CTX * HD * 2, 0x00020000);
    unsigned tn = tb + t0 + NSKIP * dt;
    u32x4 buf[8], qf;
#pragma unroll
    for (int i = 0; i < 8; ++i) {
      if (i >= 5) PACEC(tn, dt, THR(i), THR2(i)); else if (i >= NSKIP) PACE(tn, dt);
      buf[i] = __builtin_amdgcn_raw_buffer_load_b128(rk, kvo, reg ^ LOFF(i), AUX_NT);
      SB();
      if (i == 0) { qf = *(const GAS u32x4*)((const gu8*)q + qo); SB(); }
      if (i == NSKIP - 1) __builtin_amdgcn_s_barrier();
    }
    if (tid == 0) ((int*)lds)[3088 + (0)] = (int)(NOW64() - (tn - dt)) >> 6;
    const int jj = (r & 3) + 4 * (r >> 3);
    unsigned msk[4];
#pragma unroll
    for (int c = 0; c < 4; ++c) msk[c] = (c == (jj >> 1)) ? ((jj & 1) ? 0xFFFF0000u : 0x0000FFFFu) : 0u;
    asm volatile("" : "+v"(msk[0]), "+v"(msk[1]), "+v"(msk[2]), "+v"(msk[3]), "+v"(mp));
    float* sp = lds + (((lane >> 2) & 1) * 4 + wave) * 256 + h * 8;
    float m = -INFINITY;
#pragma unroll 1
    for (int b = 1; b <= 8; ++b) {
      const __amdgpu_buffer_rsrc_t rs = __builtin_amdgcn_make_buffer_rsrc((void*)(b < 8 ? args[1] : args[2]), 0, NGRP * CTX * HD * 2, 0x00020000);
      const unsigned vo = b < 8 ? kvo : vvo, bo = BOFF(b);
      float S[8];
#pragma unroll
      for (int i = 0; i < 8; ++i) {
        const f32x4 z = {0.f, 0.f, 0.f, 0.f};
        const f32x4 d = __builtin_amdgcn_mfma_f32_16x16x32_bf16(__builtin_bit_cast(bf16x8, buf[i]), __builtin_bit_cast(bf16x8, qf), z, 0, 0, 0);
        S[i] = (dpp<0x00>(d[0]) + dpp<0x55>(d[1])) + (dpp<0xAA>(d[2]) + dpp<0xFF>(d[3]));
        m = fmaxf(m, S[i]);
        asm volatile("" : "+v"(m));
        if ((i & 3) == 3) *reinterpret_cast<f32x4*>(sp + (b - 1) * 32 + (i & 4)) = f32x4{S[i - 3], S[i - 2], S[i - 1], S[i]};
        SB();
        PACEC(tn, dt, THR(i), THR2(i));
        buf[i] = __builtin_amdgcn_raw_buffer_load_b128(rs, vo, bo ^ LOFF(i), AUX_NT);
        SB();
        if (i == 5) { if (tid == 0) ((int*)lds)[3088 + (b)] = (int)(NOW64() - (tn - dt)) >> 6; SB(); }
      }
    }
    f32x4 s0 = *reinterpret_cast<const f32x4*>(sp), s1 = *reinterpret_cast<const f32x4*>(sp + 4);
    const float mc = xh_max(m) * C2;
    CFENCE();
    *mp = __uint_as_float(min(__float_as_uint(mc), EMPTY - 1u));
    f32x4 acc = {0.f, 0.f, 0.f, 0.f};
#define PV(sc, i) do { const float p_ = __builtin_amdgcn_exp2f(fmaf(sc, C2, -mc)); const unsigned pp_ = cvtpk(p_, p_); \
      const u32x4 a_ = {pp_ & msk[0], pp_ & msk[1], pp_ & msk[2], pp_ & msk[3]}; \
      acc = __builtin_amdgcn_mfma_f32_16x16x32_bf16(__builtin_bit_cast(bf16x8, a_), __builtin_bit_cast(bf16x8, buf[i]), acc, 0, 0, 0); SB(); } while (0)
#pragma unroll 1
    for (int b = 1; b < 8; ++b) {
      const unsigned bo = BOFF(b);
      const int te = b == 7 ? 0 : (LAG2 + GR2) * 64;
#pragma unroll
      for (int i = 0; i < 8; ++i) {
        PV(i < 4 ? s0[i & 3] : s1[i & 3], i);
        if (i == 3) s0 = *reinterpret_cast<const f32x4*>(sp + b * 32);
        if (i == 7) s1 = *reinterpret_cast<const f32x4*>(sp + b * 32 + 4);
        if (i >= 8 - NFREE) PACEE(tn, dt, THR(i), te); else PACEC(tn, dt, THR(i), THR2(i));
        buf[i] = __builtin_amdgcn_raw_buffer_load_b128(rv, vvo, bo ^ LOFF(i), AUX_NT);
        SB();
        if (i == 2) { if (tid == 0) ((int*)lds)[3088 + (8 + b)] = (int)(NOW64() - (tn - dt)) >> 6; SB(); }
      }
    }
#pragma unroll
    for (int i = 0; i < 8; ++i) PV(i < 4 ? s0[i & 3] : s1[i & 3], i);
    *reinterpret_cast<f32x4*>(lds_o + wave * 2 * HD + (h & 1) * HD + 8 * r + 4 * (h >> 1)) = acc;
    WG_BARRIER();
  }
  STAMP_OUT
}

extern "C" void kernel_launch(void* const* d_in, const int* in_sizes, int n_in, void* d_out, int out_size, void* d_ws, size_t ws_size,
                              hipStream_t stream) {
  tree_attn_decode<<<NGRP * NSPLIT, WAVES * 64, 0, stream>>>((const unsigned short*)d_in[0], (const unsigned short*)d_in[1], (const unsigned short*)d_in[2],
                                                             (const unsigned short*)d_in[3], (const unsigned short*)d_in[4], (const unsigned short*)d_in[5],
                                                             (unsigned short*)d_out, (unsigned char*)d_ws);
}
```

```cpp
#include <hip/hip_runtime.h>
#include <hip/hip_bf16.h>
#include <stdint.h>

typedef __attribute__((ext_vector_type(8))) __bf16 bf16x8;
typedef __attribute__((ext_vector_type(4))) float f32x4;
typedef __attribute__((ext_vector_type(2))) float f32x2;
typedef __attribute__((ext_vector_type(4))) unsigned u32x4;
typedef __attribute__((ext_vector_type(2))) unsigned u32x2;
typedef __attribute__((ext_vector_type(8))) unsigned long long u64x8;
#define GAS __attribute__((address_space(1)))
typedef GAS unsigned char gu8;
typedef GAS unsigned short gu16;
typedef GAS unsigned gu32;
typedef GAS unsigned long long gu64;
typedef __attribute__((address_space(3))) float lf32;

#define TOTAL 1706
#define HEAD 0
#define PDELAY 90
#define POWN 15
#define LAGL 40
#define LAG2 40
#define GR2 40
#define NSKIP 2
#define NFREE 4
#define DPROBE 0
#define INJC 0
#define INJR 0
#define INJS 0
#define PATC 0x40, 0x66f, 0x6d4, 0x4e5, 0x200, 0xe1, 0x2a5, 0x28, 0x2948, 0x1029, 0x33cd, 0x829, 0xc9d, 0x46c, 0x805, 0x78f
#define SPFV 0
#define STAMP(i)
#define STAMP_DECL
#define STAMP_OUT

constexpr int NB = 2, HQ = 32, HKV = 16, CTX = 8192, HD = 128;
constexpr int NGRP = NB * HKV;
constexpr int NSPLIT = 8, WAVES = 5;
constexpr float SCALE = 0.088388347648318440f, LOG2E = 1.4426950408889634f;
constexpr float C2 = SCALE * LOG2E;
constexpr int WS_O = 0, WS_ML = 262144, WS_EP = WS_ML + 65536, WS_END = WS_EP + NGRP * 4352;
constexpr int AUX_NT = 2, AUX_SC1 = 16;
constexpr unsigned PCOL[16] = {PATC};
constexpr unsigned OOB = 0x80000000u, EMPTY = 0xFFFFFFFFu;

__device__ __forceinline__ unsigned cvtpk(float lo, float hi) {
  typedef __attribute__((ext_vector_type(2))) __bf16 bf16x2;
  f32x2 v = {lo, hi};
  return __builtin_bit_cast(unsigned, __builtin_convertvector(v, bf16x2));
}
template <int CTRL> __device__ __forceinline__ float dpp(float x) {
  return __builtin_bit_cast(float, __builtin_amdgcn_mov_dpp(__builtin_bit_cast(int, x), CTRL, 0xf, 0xf, true));
}
template <int I> __device__ __forceinline__ float rowb(float x) { return dpp<0x150 + I>(x); }
__device__ __forceinline__ float x16_sum(float x) {
  auto s = __builtin_amdgcn_permlane16_swap(__float_as_uint(x), __float_as_uint(x), false, false);
  return __uint_as_float(s[0]) + __uint_as_float(s[1]);
}
__device__ __forceinline__ float xh_max(float x) {
  auto s = __builtin_amdgcn_permlane16_swap(__float_as_uint(x), __float_as_uint(x), false, false);
  x = fmaxf(__uint_as_float(s[0]), __uint_as_float(s[1]));
  auto t = __builtin_amdgcn_permlane32_swap(__float_as_uint(x), __float_as_uint(x), false, false);
  return fmaxf(__uint_as_float(t[0]), __uint_as_float(t[1]));
}
__device__ __forceinline__ float max8(float x) { x = fmaxf(x, dpp<0xB1>(x)); x = fmaxf(x, dpp<0x4E>(x)); return fmaxf(x, dpp<0x141>(x)); }
__device__ __forceinline__ float sum8(float x) { x += dpp<0xB1>(x); x += dpp<0x4E>(x); return x + dpp<0x141>(x); }
__device__ __forceinline__ float sum16(float x) { x = sum8(x); return x + dpp<0x128>(x); }
__device__ __forceinline__ float lo16(unsigned x) { return __uint_as_float(x << 16); }
__device__ __forceinline__ float hi16(unsigned x) { return __uint_as_float(x & 0xFFFF0000u); }
__device__ __forceinline__ unsigned umax4(u32x4 v) { return max(max(v[0], v[1]), max(v[2], v[3])); }
#define RLX_AGENT __ATOMIC_RELAXED, __HIP_MEMORY_SCOPE_AGENT
#define RLX_WG __ATOMIC_RELAXED, __HIP_MEMORY_SCOPE_WORKGROUP
#define CFENCE() asm volatile("" ::: "memory")
#define SB() __builtin_amdgcn_sched_barrier(0)
#define WG_BARRIER() asm volatile("s_waitcnt lgkmcnt(0)\n\ts_barrier" ::: "memory")
#define PACE_TXT "1:\n\ts_memrealtime vcc\n\ts_waitcnt lgkmcnt(0)\n\ts_lshl_b32 vcc_lo, vcc_lo, 6\n\t" \
    "s_sub_i32 vcc_lo, vcc_lo, %0\n\ts_cmp_lt_i32 vcc_lo, 0\n\ts_cbranch_scc1 1b\n\ts_add_i32 %0, %0, %1"
#define PACE(t, d) asm volatile(PACE_TXT : "+s"(t) : "s"(d) : "vcc", "scc", "memory")
#define PACEC(t, d, thr, thr2) asm volatile(PACE_TXT "\n\ts_cmpk_ge_i32 vcc_lo, %3\n\ts_cbranch_scc1 3f\n\ts_cmpk_ge_i32 vcc_lo, %2\n\ts_cbranch_scc1 2f\n\t" \
    "s_waitcnt vmcnt(4)\n2:\n\ts_waitcnt vmcnt(5)\n3:\n\ts_waitcnt vmcnt(6)" : "+s"(t) : "s"(d), "i"(thr), "i"(thr2) : "vcc", "scc", "memory")
#define PACEE(t, d, thr, thr2) asm volatile(PACE_TXT "\n\ts_cmp_ge_i32 vcc_lo, %3\n\ts_cbranch_scc1 3f\n\ts_cmpk_ge_i32 vcc_lo, %2\n\ts_cbranch_scc1 2f\n\t" \
    "s_waitcnt vmcnt(4)\n2:\n\ts_waitcnt vmcnt(5)\n3:\n\ts_waitcnt vmcnt(6)" : "+s"(t) : "s"(d), "i"(thr), "s"(thr2) : "vcc", "scc", "memory")
#define ORD(i) (((i) & 3) == 0 ? 1 : ((i) & 3) == 1 ? 3 : ((i) & 3) == 2 ? 2 : 4)
#define THR(i) (LAGL * 16 * ORD(i))
#define THR2(i) (LAG2 * 64 + GR2 * 16 * ORD(i))
#define NOW64() ((unsigned)__builtin_amdgcn_s_memrealtime() << 6)
#define NAP(t) do { const unsigned t_ = (t); while ((int)(NOW64() - t_) < 0) __builtin_amdgcn_s_sleep(4); } while (0)

__global__ void __launch_bounds__(WAVES * 64)
tree_attn_decode(const unsigned short*, const unsigned short*, const unsigned short*, const unsigned short*, const unsigned short*,
                 const unsigned short*, unsigned short*, unsigned char*) {
  u64x8 args;
  unsigned long long tb64;
  asm volatile("s_load_dwordx16 %0, %2, 0x0\n\ts_memrealtime %1" : "=&s"(args), "=&s"(tb64) : "s"(__builtin_amdgcn_kernarg_segment_ptr()));
  SB();
  __shared__ __attribute__((aligned(16))) float lds[2048 + 1024 + 16 + 16];
  float* lds_o = lds + 2048;
  float* lds_m = lds_o + 1024;
  STAMP_DECL
  STAMP(0)
  const int bid = blockIdx.x, g = bid >> 3, s = bid & 7;
  const int tid = threadIdx.x, lane = tid & 63, wave = __builtin_amdgcn_readfirstlane(tid >> 6);
  const int r = lane & 15, h = lane >> 4;
  if (wave >= 4) {
    unsigned long long pc;
    unsigned j0, j1, j2, j3;
    asm volatile("s_getpc_b64 %0" : "=s"(pc));
    asm volatile("s_load_dword %0, %4, 0x0\n\ts_load_dword %1, %4, 0x40\n\ts_load_dword %2, %4, 0x80\n\ts_load_dword %3, %4, 0xc0\n\t"
                 "s_barrier\n\ts_waitcnt lgkmcnt(0)"
                 : "=&s"(j0), "=&s"(j1), "=&s"(j2), "=&s"(j3) : "s"((pc & ~255ull) + g * 256) : "memory");
  }
  const unsigned kvo = (r >> 2) * 256 + h * 64 + (r & 3) * 16, vvo = lane * 16;
  unsigned c0 = 0;
#pragma unroll
  for (int b = 0; b < 2; ++b) c0 ^= ((wave >> b) & 1) ? PCOL[6 + b] : 0u;
#pragma unroll
  for (int b = 0; b < 8; ++b) c0 ^= ((bid >> b) & 1) ? PCOL[8 + b] : 0u;
  const unsigned reg = ((c0 & 0x7FF) << 10) | (g << 21);
#define LOFF(i) (((((i) & 1) ? PCOL[0] : 0u) ^ (((i) & 2) ? PCOL[1] : 0u) ^ (((i) & 4) ? PCOL[2] : 0u)) << 10)
#define BOFF(b) (reg ^ (((((b) & 1) ? PCOL[3] : 0u) ^ (((b) & 2) ? PCOL[4] : 0u) ^ (((b) & 4) ? PCOL[5] : 0u)) << 10))
  const unsigned t0 = ((wave * 8 + (c0 >> 11)) * TOTAL >> 6) + HEAD * 64, dt = TOTAL / 2;
  const unsigned qo = (g * 2 + ((r >> 2) & 1)) * 256 + (4 * h + (r & 3)) * 16;
  lf32* mp = (lf32*)(lds_m + (lane & 4) + (wave & 3) + (wave & 4) * 2);
  *mp = __uint_as_float(EMPTY);
  asm volatile("" :: "s"(t0), "s"(reg), "v"(kvo), "v"(qo) : "memory");
  SB();
  asm volatile("s_waitcnt lgkmcnt(0)" : "+s"(args), "+s"(tb64));
  const gu16* q = (const gu16*)args[0];
  gu8* ws = (gu8*)args[7];
  unsigned tb = (unsigned)tb64 << 6;
  if (INJS) tb += ((((s + 8 * (tb >> 18)) * 2654435761u) >> 16) % (unsigned)(INJS + 1)) * 64;
  if (wave >= 4) {
    const __amdgpu_buffer_rsrc_t rw = __builtin_amdgcn_make_buffer_rsrc((void*)ws, 0, WS_END, 0x00020000);
    NAP(tb + 250 * 64);
    if (SPFV) { unsigned j0, j1; asm volatile("s_load_dword %0, %2, 0x0\n\ts_load_dword %1, %3, 0x0\n\ts_waitcnt lgkmcnt(0)" : "=&s"(j0), "=&s"(j1) : "s"(args[2] + reg), "s"(args[7] + bid * 1024) : "memory"); }
    if (SPFV == 2) { const unsigned jv = *(const volatile GAS unsigned*)(args[2] + reg); asm volatile("" :: "v"(jv)); }
    const unsigned tag = __hip_atomic_load((gu32*)(ws + WS_EP + g * 4352), RLX_AGENT) + 1u;
    __builtin_amdgcn_raw_buffer_store_b128(u32x4{EMPTY, EMPTY, EMPTY, EMPTY}, rw, lane * 16, WS_O + bid * 1024, AUX_SC1);
    float st = 0.f;
    u32x2 vv = {0u, 0u};
    if (s == 0) {
      const u32x2 kk = *(const GAS u32x2*)((const gu16*)args[3] + g * HD + 4 * (lane & 31));
      const u32x2 qq = *(const GAS u32x2*)(q + g * 2 * HD + 4 * lane);
      vv = *(const GAS u32x2*)((const gu16*)args[4] + g * HD + 4 * (lane & 31));
      const float bias = (1.0f - lo16(((const gu16*)args[5])[g / HKV])) * -1000000000.0f;
      st = (lo16(kk[0]) * lo16(qq[0]) + hi16(kk[0]) * hi16(qq[0])) + (lo16(kk[1]) * lo16(qq[1]) + hi16(kk[1]) * hi16(qq[1]));
      st = (x16_sum(sum16(st)) * SCALE + bias) * LOG2E;
    }
    asm volatile("s_waitcnt vmcnt(0)" ::: "memory");
    f32x4 w4;
    do {
      __builtin_amdgcn_s_sleep(8);
      CFENCE();
      w4 = *reinterpret_cast<const f32x4*>(lds_m + (lane >> 5) * 4);
    } while (__builtin_amdgcn_uicmp(umax4(__builtin_bit_cast(u32x4, w4)), EMPTY, 32));
    CFENCE();
    STAMP(1)
    const float M = fmaxf(fmaxf(w4[0], w4[1]), fmaxf(w4[2], w4[3]));
#pragma unroll
    for (int c = 0; c < 4; ++c) w4[c] = __builtin_amdgcn_exp2f(w4[c] - M);
    float L = 0.f;
#pragma unroll 1
    for (int k = 0; k < 8; ++k) {
      const f32x4 x = *reinterpret_cast<const f32x4*>(lds + (lane >> 5) * 1024 + k * 128 + (lane & 31) * 4);
      L += (__builtin_amdgcn_exp2f(fmaf(x[0], C2, -M)) + __builtin_amdgcn_exp2f(fmaf(x[1], C2, -M))) +
           (__builtin_amdgcn_exp2f(fmaf(x[2], C2, -M)) + __builtin_amdgcn_exp2f(fmaf(x[3], C2, -M)));
    }
    L = x16_sum(sum16(L));
#define REC(i) (*reinterpret_cast<const f32x4*>(lds_o + (i) * 2 * HD + 4 * lane))
    if (s != 0) {
      const u32x4 gm = {(lane & 1) ? tb : __float_as_uint(M), tag, __float_as_uint(L), tag};
      __builtin_amdgcn_raw_buffer_store_b128(gm, rw, (lane & 24) == 0 ? (lane >> 5) * 128 + (lane & 7) * 16 : OOB, WS_ML + bid * 256, AUX_SC1);
      STAMP(3)
      WG_BARRIER();
      STAMP(4)
      u32x4 go = __builtin_bit_cast(u32x4, (w4[0] * REC(0) + w4[1] * REC(1)) + (w4[2] * REC(2) + w4[3] * REC(3)));
#pragma unroll
      for (int c = 0; c < 4; ++c) go[c] = min(go[c], EMPTY - 1u);
      __builtin_amdgcn_raw_buffer_store_b128(go, rw, lane * 16, WS_O + bid * 1024, AUX_SC1);
    } else {
      NAP(NOW64() + 150 * 64);
      u32x4 a;
      for (;;) {
        CFENCE();
        a = __builtin_amdgcn_raw_buffer_load_b128(rw, (lane & 7) * 256 + (lane >> 5) * 128 + (lane & 8) * 2, WS_ML + bid * 256, AUX_SC1);
        if (((__builtin_amdgcn_uicmp(a[1], tag, 33) | __builtin_amdgcn_uicmp(a[3], tag, 33)) & 0xFEFEFEFEFEFEFEFEull) == 0) break;
        __builtin_amdgcn_s_sleep(16);
      }
      if (lane == 0) __hip_atomic_store((gu32*)(ws + WS_EP + g * 4352), tag, RLX_AGENT);
      const float mk = (lane & 7) == 0 ? M : __uint_as_float(a[0]), lk = (lane & 7) == 0 ? L : __uint_as_float(a[2]);
      const float Mx = max8(fmaxf(mk, st));
      const float wk = __builtin_amdgcn_exp2f(mk - Mx), wt = __builtin_amdgcn_exp2f(st - Mx);
      const float inv = 1.0f / (sum8(wk * lk) + wt), ck = wk * inv;
      w4 *= rowb<0>(ck);
      const float c1 = rowb<1>(ck), c2 = rowb<2>(ck), c3 = rowb<3>(ck), c4 = rowb<4>(ck), c5 = rowb<5>(ck), c6 = rowb<6>(ck), c7 = rowb<7>(ck);
      f32x4 o = rowb<0>(wt * inv) * f32x4{lo16(vv[0]), hi16(vv[0]), lo16(vv[1]), hi16(vv[1])};
      const float skew = max8((lane & 7) == 0 ? 0.f : (float)(int)(a[0] - tb));
      const unsigned te = tb + (HEAD + TOTAL + PDELAY) * 64 + __builtin_amdgcn_readlane((int)skew, 8);
      gu64* op = (gu64*)((gu16*)args[6] + g * 2 * HD + 4 * lane);
      STAMP(3)
      WG_BARRIER();
      STAMP(4)
      unsigned tp = NOW64() + POWN * 64;
      o += (w4[0] * REC(0) + w4[1] * REC(1)) + (w4[2] * REC(2) + w4[3] * REC(3));
      if ((int)(te - tp) > 0) tp = te;
      PACE(tp, dt);
      u32x4 po[NSPLIT];
      unsigned mx;
      do {
        CFENCE();
#pragma unroll
        for (int k = 1; k < NSPLIT; ++k) po[k] = __builtin_amdgcn_raw_buffer_load_b128(rw, lane * 16 + (k & 3) * 1024, WS_O + bid * 1024 + (k >> 2) * 4096, AUX_SC1);
        mx = umax4(po[1]);
#pragma unroll
        for (int k = 2; k < NSPLIT; ++k) mx = max(mx, umax4(po[k]));
      } while (__builtin_amdgcn_uicmp(mx, EMPTY, 32));
#define PO(k) __builtin_bit_cast(f32x4, po[k])
      o = ((o + c1 * PO(1)) + (c2 * PO(2) + c3 * PO(3))) + ((c4 * PO(4) + c5 * PO(5)) + (c6 * PO(6) + c7 * PO(7)));
      __hip_atomic_store(op, (unsigned long long)cvtpk(o[0], o[1]) | ((unsigned long long)cvtpk(o[2], o[3]) << 32), RLX_AGENT);
      if (DPROBE) { asm volatile("s_waitcnt vmcnt(0)" ::: "memory"); const unsigned until = tb + 3000 * 64 + 5 * (NOW64() - tb - 1800 * 64); while ((int)(NOW64() - until) < 0) __builtin_amdgcn_s_sleep(1); }
    }
    {
      const unsigned tend = NOW64();
      NAP(tb + 2600 * 64);
      gu32* dg = (gu32*)(ws + (1 << 20));
      if (lane < 16) __hip_atomic_store(dg + bid * 32 + lane, ((unsigned*)lds)[3088 + lane], RLX_AGENT);
      if (lane == 16) __hip_atomic_store(dg + bid * 32 + 16, tb, RLX_AGENT);
      if (lane == 17) __hip_atomic_store(dg + bid * 32 + 17, tend, RLX_AGENT);
      if (bid == 0) {
        NAP(tb + 3200 * 64);
        float sm = 0.f, mx = -1e9f;
        for (int k = 0; k < 64; ++k) { const float v = (float)(int)__hip_atomic_load(dg + ((lane >> 4) * 64 + k) * 32 + (lane & 15), RLX_AGENT); sm += v; mx = fmaxf(mx, v); }
        sm = x16_sum(sm); { auto t = __builtin_amdgcn_permlane32_swap(__float_as_uint(sm), __float_as_uint(sm), false, false); sm = __uint_as_float(t[0]) + __uint_as_float(t[1]); }
        mx = xh_max(mx);
        float st_ = 0.f, l15 = 0.f, en = -1e9f;
        for (int k = 0; k < 32; ++k) { const int bb = k * 8 + (lane & 7);
          st_ += (float)((int)(__hip_atomic_load(dg + bb * 32 + 16, RLX_AGENT) - tb) >> 6) * (1.f / 32); l15 += (float)(int)__hip_atomic_load(dg + bb * 32 + 14, RLX_AGENT) * (1.f / 32);
          en = fmaxf(en, -(float)(int)__hip_atomic_load(dg + bb * 32 + 14, RLX_AGENT)); }
        unsigned long long pk[10];
        for (int w = 0; w < 10; ++w) pk[w] = 0;
#pragma unroll
        for (int k = 0; k < 16; ++k) { pk[k >> 3] |= (unsigned long long)(min(__builtin_amdgcn_readlane((int)(sm * (1.f / 256)), k), 255) & 255) << (8 * (7 - (k & 7)));
                                       pk[2 + (k >> 3)] |= (unsigned long long)(min(__builtin_amdgcn_readlane((int)mx, k), 255) & 255) << (8 * (7 - (k & 7))); }
#pragma unroll
        for (int k = 0; k < 8; ++k) { pk[4] |= (unsigned long long)((__builtin_amdgcn_readlane((int)st_, k) + 64) & 255) << (8 * (7 - k)); pk[5] |= (unsigned long long)(min(__builtin_amdgcn_readlane((int)l15, k), 255) & 255) << (8 * (7 - k));
                                      pk[6] |= (unsigned long long)(min(__builtin_amdgcn_readlane((int)-en, k), 255) & 255) << (8 * (7 - k)); }
        if (lane == 0 && ((pk[0] >> 56) >= 0x30 && (pk[2] >> 56) * 10 <= (pk[0] >> 56) * 22)) {
          auto msg = __ockl_fprintf_stderr_begin();
          msg = __ockl_fprintf_append_string_n(msg, "D %016lx%016lx %016lx%016lx %016lx %016lx %016lx\n", 50, 0);
          __ockl_fprintf_append_args(msg, 7, pk[0], pk[1], pk[2], pk[3], pk[4], pk[5], pk[6], 1);
          if (((tb >> 8) * 2654435761u >> 16) % 300 == 999) ((gu16*)args[6])[0] = 0x7000;
        }
      }
    }
  } else {
    if (INJC | INJR | INJS) { const unsigned dl = 25 + INJC + (INJR ? (((bid + (tb >> 18)) * 2654435761u) >> 16) % (unsigned)(INJR + 1) : 0u); while ((int)(NOW64() - tb) < (int)dl * 64) {} }
    const __amdgpu_buffer_rsrc_t rk = __builtin_amdgcn_make_buffer_rsrc((void*)args[1], 0, NGRP * CTX * HD * 2, 0x00020000);
    const __amdgpu_buffer_rsrc_t rv = __builtin_amdgcn_make_buffer_rsrc((void*)args[2], 0, NGRP * CTX * HD * 2, 0x00020000);
    const int tka = (int)(NOW64() - tb) >> 6;
    unsigned tn = tb + t0 + NSKIP * dt;
    u32x4 buf[8], qf;
#pragma unroll
    for (int i = 0; i < 8; ++i) {
      if (i >= 5) PACEC(tn, dt, THR(i), THR2(i)); else if (i >= NSKIP) PACE(tn, dt);
      buf[i] = __builtin_amdgcn_raw_buffer_load_b128(rk, kvo, reg ^ LOFF(i), AUX_NT);
      SB();
      if (i == 0) { qf = *(const GAS u32x4*)((const gu8*)q + qo); SB(); }
      if (i == NSKIP - 1) __builtin_amdgcn_s_barrier();
      if (i == 4) { asm volatile("s_waitcnt vmcnt(5)" ::: "memory"); const int t1 = (int)(NOW64() - tb) >> 6; asm volatile("s_waitcnt vmcnt(4)" ::: "memory"); const int t2 = (int)(NOW64() - tb) >> 6;
        if (tid == 0) { ((int*)lds)[3088 + 13] = tka; ((int*)lds)[3088 + 14] = t1; ((int*)lds)[3088 + 15] = t2; } SB(); }
    }
    if (tid == 0) ((int*)lds)[3088 + (0)] = (int)(NOW64() - (tn - dt)) >> 6;
    const int jj = (r & 3) + 4 * (r >> 3);
    unsigned msk[4];
#pragma unroll
    for (int c = 0; c < 4; ++c) msk[c] = (c == (jj >> 1)) ? ((jj & 1) ? 0xFFFF0000u : 0x0000FFFFu) : 0u;
    asm volatile("" : "+v"(msk[0]), "+v"(msk[1]), "+v"(msk[2]), "+v"(msk[3]), "+v"(mp));
    float* sp = lds + (((lane >> 2) & 1) * 4 + wave) * 256 + h * 8;
    float m = -INFINITY;
#pragma unroll 1
    for (int b = 1; b <= 8; ++b) {
      const __amdgpu_buffer_rsrc_t rs = __builtin_amdgcn_make_buffer_rsrc((void*)(b < 8 ? args[1] : args[2]), 0, NGRP * CTX * HD * 2, 0x00020000);
      const unsigned vo = b < 8 ? kvo : vvo, bo = BOFF(b);
      float S[8];
#pragma unroll
      for (int i = 0; i < 8; ++i) {
        const f32x4 z = {0.f, 0.f, 0.f, 0.f};
        const f32x4 d = __builtin_amdgcn_mfma_f32_16x16x32_bf16(__builtin_bit_cast(bf16x8, buf[i]), __builtin_bit_cast(bf16x8, qf), z, 0, 0, 0);
        S[i] = (dpp<0x00>(d[0]) + dpp<0x55>(d[1])) + (dpp<0xAA>(d[2]) + dpp<0xFF>(d[3]));
        m = fmaxf(m, S[i]);
        asm volatile("" : "+v"(m));
        if ((i & 3) == 3) *reinterpret_cast<f32x4*>(sp + (b - 1) * 32 + (i & 4)) = f32x4{S[i - 3], S[i - 2], S[i - 1], S[i]};
        SB();
        PACEC(tn, dt, THR(i), THR2(i));
        buf[i] = __builtin_amdgcn_raw_buffer_load_b128(rs, vo, bo ^ LOFF(i), AUX_NT);
        SB();
        if (i == 5) { if (tid == 0) ((int*)lds)[3088 + (b)] = (int)(NOW64() - (tn - dt)) >> 6; SB(); }
      }
    }
    f32x4 s0 = *reinterpret_cast<const f32x4*>(sp), s1 = *reinterpret_cast<const f32x4*>(sp + 4);
    const float mc = xh_max(m) * C2;
    CFENCE();
    *mp = __uint_as_float(min(__float_as_uint(mc), EMPTY - 1u));
    f32x4 acc = {0.f, 0.f, 0.f, 0.f};
#define PV(sc, i) do { const float p_ = __builtin_amdgcn_exp2f(fmaf(sc, C2, -mc)); const unsigned pp_ = cvtpk(p_, p_); \
      const u32x4 a_ = {pp_ & msk[0], pp_ & msk[1], pp_ & msk[2], pp_ & msk[3]}; \
      acc = __builtin_amdgcn_mfma_f32_16x16x32_bf16(__builtin_bit_cast(bf16x8, a_), __builtin_bit_cast(bf16x8, buf[i]), acc, 0, 0, 0); SB(); } while (0)
#pragma unroll 1
    for (int b = 1; b < 8; ++b) {
      const unsigned bo = BOFF(b);
      const int te = b == 7 ? 0 : (LAG2 + GR2) * 64;
#pragma unroll
      for (int i = 0; i < 8; ++i) {
        PV(i < 4 ? s0[i & 3] : s1[i & 3], i);
        if (i == 3) s0 = *reinterpret_cast<const f32x4*>(sp + b * 32);
        if (i == 7) s1 = *reinterpret_cast<const f32x4*>(sp + b * 32 + 4);
        if (i >= 8 - NFREE) PACEE(tn, dt, THR(i), te); else PACEC(tn, dt, THR(i), THR2(i));
        buf[i] = __builtin_amdgcn_raw_buffer_load_b128(rv, vvo, bo ^ LOFF(i), AUX_NT);
        SB();
        if (i == 2) { if (tid == 0 && b < 5) ((int*)lds)[3088 + 8 + b] = (int)(NOW64() - (tn - dt)) >> 6; SB(); }
      }
    }
#pragma unroll
    for (int i = 0; i < 8; ++i) PV(i < 4 ? s0[i & 3] : s1[i & 3], i);
    *reinterpret_cast<f32x4*>(lds_o + wave * 2 * HD + (h & 1) * HD + 8 * r + 4 * (h >> 1)) = acc;
    WG_BARRIER();
  }
  STAMP_OUT
}

extern "C" void kernel_launch(void* const* d_in, const int* in_sizes, int n_in, void* d_out, int out_size, void* d_ws, size_t ws_size,
                              hipStream_t stream) {
  tree_attn_decode<<<NGRP * NSPLIT, WAVES * 64, 0, stream>>>((const unsigned short*)d_in[0], (const unsigned short*)d_in[1], (const unsigned short*)d_in[2],
                                                             (const unsigned short*)d_in[3], (const unsigned short*)d_in[4], (const unsigned short*)d_in[5],
                                                             (unsigned short*)d_out, (unsigned char*)d_ws);
}
```

```cpp
#include <hip/hip_runtime.h>
#include <hip/hip_bf16.h>
#include <stdint.h>

typedef __attribute__((ext_vector_type(8))) __bf16 bf16x8;
typedef __attribute__((ext_vector_type(4))) float f32x4;
typedef __attribute__((ext_vector_type(2))) float f32x2;
typedef __attribute__((ext_vector_type(4))) unsigned u32x4;
typedef __attribute__((ext_vector_type(2))) unsigned u32x2;
typedef __attribute__((ext_vector_type(8))) unsigned long long u64x8;
#define GAS __attribute__((address_space(1)))
typedef GAS unsigned char gu8;
typedef GAS unsigned short gu16;
typedef GAS unsigned gu32;
typedef GAS unsigned long long gu64;
typedef __attribute__((address_space(3))) float lf32;

#define TOTAL 1696
#define HEAD 0
#define PDELAY 90
#define POWN 15
#define LAGL 40
#define LAG2 40
#define GR2 40
#define NSKIP 2
#define NFREE 4
#define DPROBE 1
#define INJC 0
#define INJR 0
#define INJS 0
#define PATC 0x40, 0x66f, 0x6d4, 0x4e5, 0x200, 0xe1, 0x2a5, 0x28, 0x2948, 0x1029, 0x33cd, 0x829, 0xc9d, 0x46c, 0x805, 0x78f
#define SPFV 0
#define KSLIP 36
#define STAMP(i)
#define STAMP_DECL
#define STAMP_OUT

constexpr int NB = 2, HQ = 32, HKV = 16, CTX = 8192, HD = 128;
constexpr int NGRP = NB * HKV;
constexpr int NSPLIT = 8, WAVES = 5;
constexpr float SCALE = 0.088388347648318440f, LOG2E = 1.4426950408889634f;
constexpr float C2 = SCALE * LOG2E;
constexpr int WS_O = 0, WS_ML = 262144, WS_EP = WS_ML + 65536, WS_END = WS_EP + NGRP * 4352;
constexpr int AUX_NT = 2, AUX_SC1 = 16;
constexpr unsigned PCOL[16] = {PATC};
constexpr unsigned OOB = 0x80000000u, EMPTY = 0xFFFFFFFFu;

__device__ __forceinline__ unsigned cvtpk(float lo, float hi) {
  typedef __attribute__((ext_vector_type(2))) __bf16 bf16x2;
  f32x2 v = {lo, hi};
  return __builtin_bit_cast(unsigned, __builtin_convertvector(v, bf16x2));
}
template <int CTRL> __device__ __forceinline__ float dpp(float x) {
  return __builtin_bit_cast(float, __builtin_amdgcn_mov_dpp(__builtin_bit_cast(int, x), CTRL, 0xf, 0xf, true));
}
template <int I> __device__ __forceinline__ float rowb(float x) { return dpp<0x150 + I>(x); }
__device__ __forceinline__ float x16_sum(float x) {
  auto s = __builtin_amdgcn_permlane16_swap(__float_as_uint(x), __float_as_uint(x), false, false);
  return __uint_as_float(s[0]) + __uint_as_float(s[1]);
}
__device__ __forceinline__ float xh_max(float x) {
  auto s = __builtin_amdgcn_permlane16_swap(__float_as_uint(x), __float_as_uint(x), false, false);
  x = fmaxf(__uint_as_float(s[0]), __uint_as_float(s[1]));
  auto t = __builtin_amdgcn_permlane32_swap(__float_as_uint(x), __float_as_uint(x), false, false);
  return fmaxf(__uint_as_float(t[0]), __uint_as_float(t[1]));
}
__device__ __forceinline__ float max8(float x) { x = fmaxf(x, dpp<0xB1>(x)); x = fmaxf(x, dpp<0x4E>(x)); return fmaxf(x, dpp<0x141>(x)); }
__device__ __forceinline__ float sum8(float x) { x += dpp<0xB1>(x); x += dpp<0x4E>(x); return x + dpp<0x141>(x); }
__device__ __forceinline__ float sum16(float x) { x = sum8(x); return x + dpp<0x128>(x); }
__device__ __forceinline__ float lo16(unsigned x) { return __uint_as_float(x << 16); }
__device__ __forceinline__ float hi16(unsigned x) { return __uint_as_float(x & 0xFFFF0000u); }
__device__ __forceinline__ unsigned umax4(u32x4 v) { return max(max(v[0], v[1]), max(v[2], v[3])); }
#define RLX_AGENT __ATOMIC_RELAXED, __HIP_MEMORY_SCOPE_AGENT
#define RLX_WG __ATOMIC_RELAXED, __HIP_MEMORY_SCOPE_WORKGROUP
#define CFENCE() asm volatile("" ::: "memory")
#define SB() __builtin_amdgcn_sched_barrier(0)
#define WG_BARRIER() asm volatile("s_waitcnt lgkmcnt(0)\n\ts_barrier" ::: "memory")
#define PACE_TXT "1:\n\ts_memrealtime vcc\n\ts_waitcnt lgkmcnt(0)\n\ts_lshl_b32 vcc_lo, vcc_lo, 6\n\t" \
    "s_sub_i32 vcc_lo, vcc_lo, %0\n\ts_cmp_lt_i32 vcc_lo, 0\n\ts_cbranch_scc1 1b\n\ts_add_i32 %0, %0, %1"
#define PACE(t, d) asm volatile(PACE_TXT : "+s"(t) : "s"(d) : "vcc", "scc", "memory")
#define PACEC(t, d, thr, thr2) asm volatile(PACE_TXT "\n\ts_cmpk_ge_i32 vcc_lo, %3\n\ts_cbranch_scc1 3f\n\ts_cmpk_ge_i32 vcc_lo, %2\n\ts_cbranch_scc1 2f\n\t" \
    "s_waitcnt vmcnt(4)\n2:\n\ts_waitcnt vmcnt(5)\n3:\n\ts_waitcnt vmcnt(6)" : "+s"(t) : "s"(d), "i"(thr), "i"(thr2) : "vcc", "scc", "memory")
#define PACEE(t, d, thr, thr2) asm volatile(PACE_TXT "\n\ts_cmp_ge_i32 vcc_lo, %3\n\ts_cbranch_scc1 3f\n\ts_cmpk_ge_i32 vcc_lo, %2\n\ts_cbranch_scc1 2f\n\t" \
    "s_waitcnt vmcnt(4)\n2:\n\ts_waitcnt vmcnt(5)\n3:\n\ts_waitcnt vmcnt(6)" : "+s"(t) : "s"(d), "i"(thr), "s"(thr2) : "vcc", "scc", "memory")
#define ORD(i) (((i) & 3) == 0 ? 1 : ((i) & 3) == 1 ? 3 : ((i) & 3) == 2 ? 2 : 4)
#define THR(i) (LAGL * 16 * ORD(i))
#define THR2(i) (LAG2 * 64 + GR2 * 16 * ORD(i))
#define NOW64() ((unsigned)__builtin_amdgcn_s_memrealtime() << 6)
#define NAP(t) do { const unsigned t_ = (t); while ((int)(NOW64() - t_) < 0) __builtin_amdgcn_s_sleep(4); } while (0)

__global__ void __launch_bounds__(WAVES * 64)
tree_attn_decode(const unsigned short*, const unsigned short*, const unsigned short*, const unsigned short*, const unsigned short*,
                 const unsigned short*, unsigned short*, unsigned char*) {
  u64x8 args;
  unsigned long long tb64;
  asm volatile("s_load_dwordx16 %0, %2, 0x0\n\ts_memrealtime %1" : "=&s"(args), "=&s"(tb64) : "s"(__builtin_amdgcn_kernarg_segment_ptr()));
  SB();
  __shared__ __attribute__((aligned(16))) float lds[2048 + 1024 + 16];
  float* lds_o = lds + 2048;
  float* lds_m = lds_o + 1024;
  STAMP_DECL
  STAMP(0)
  const int bid = blockIdx.x, g = bid >> 3, s = bid & 7;
  const int tid = threadIdx.x, lane = tid & 63, wave = __builtin_amdgcn_readfirstlane(tid >> 6);
  const int r = lane & 15, h = lane >> 4;
  if (wave >= 4) {
    unsigned long long pc;
    unsigned j0, j1, j2, j3;
    asm volatile("s_getpc_b64 %0" : "=s"(pc));
    asm volatile("s_load_dword %0, %4, 0x0\n\ts_load_dword %1, %4, 0x40\n\ts_load_dword %2, %4, 0x80\n\ts_load_dword %3, %4, 0xc0\n\t"
                 "s_barrier\n\ts_waitcnt lgkmcnt(0)"
                 : "=&s"(j0), "=&s"(j1), "=&s"(j2), "=&s"(j3) : "s"((pc & ~255ull) + g * 256) : "memory");
  }
  const unsigned kvo = (r >> 2) * 256 + h * 64 + (r & 3) * 16, vvo = lane * 16;
  unsigned c0 = 0;
#pragma unroll
  for (int b = 0; b < 2; ++b) c0 ^= ((wave >> b) & 1) ? PCOL[6 + b] : 0u;
#pragma unroll
  for (int b = 0; b < 8; ++b) c0 ^= ((bid >> b) & 1) ? PCOL[8 + b] : 0u;
  const unsigned reg = ((c0 & 0x7FF) << 10) | (g << 21);
#define LOFF(i) (((((i) & 1) ? PCOL[0] : 0u) ^ (((i) & 2) ? PCOL[1] : 0u) ^ (((i) & 4) ? PCOL[2] : 0u)) << 10)
#define BOFF(b) (reg ^ (((((b) & 1) ? PCOL[3] : 0u) ^ (((b) & 2) ? PCOL[4] : 0u) ^ (((b) & 4) ? PCOL[5] : 0u)) << 10))
  const unsigned t0 = ((wave * 8 + (c0 >> 11)) * TOTAL >> 6) + HEAD * 64, dt = TOTAL / 2;
  const unsigned qo = (g * 2 + ((r >> 2) & 1)) * 256 + (4 * h + (r & 3)) * 16;
  lf32* mp = (lf32*)(lds_m + (lane & 4) + (wave & 3) + (wave & 4) * 2);
  *mp = __uint_as_float(EMPTY);
  asm volatile("" :: "s"(t0), "s"(reg), "v"(kvo), "v"(qo) : "memory");
  SB();
  asm volatile("s_waitcnt lgkmcnt(0)" : "+s"(args), "+s"(tb64));
  const gu16* q = (const gu16*)args[0];
  gu8* ws = (gu8*)args[7];
  unsigned tb = (unsigned)tb64 << 6;
  if (INJS) tb += ((((s + 8 * (tb >> 18)) * 2654435761u) >> 16) % (unsigned)(INJS + 1)) * 64;
  if (wave >= 4) {
    const __amdgpu_buffer_rsrc_t rw = __builtin_amdgcn_make_buffer_rsrc((void*)ws, 0, WS_END, 0x00020000);
    NAP(tb + 250 * 64);
    if (SPFV) { unsigned j0, j1; asm volatile("s_load_dword %0, %2, 0x0\n\ts_load_dword %1, %3, 0x0\n\ts_waitcnt lgkmcnt(0)" : "=&s"(j0), "=&s"(j1) : "s"(args[2] + reg), "s"(args[7] + bid * 1024) : "memory"); }
    if (SPFV == 2) { const unsigned jv = *(const volatile GAS unsigned*)(args[2] + reg); asm volatile("" :: "v"(jv)); }
    const unsigned tag = __hip_atomic_load((gu32*)(ws + WS_EP + g * 4352), RLX_AGENT) + 1u;
    __builtin_amdgcn_raw_buffer_store_b128(u32x4{EMPTY, EMPTY, EMPTY, EMPTY}, rw, lane * 16, WS_O + bid * 1024, AUX_SC1);
    float st = 0.f;
    u32x2 vv = {0u, 0u};
    if (s == 0) {
      const u32x2 kk = *(const GAS u32x2*)((const gu16*)args[3] + g * HD + 4 * (lane & 31));
      const u32x2 qq = *(const GAS u32x2*)(q + g * 2 * HD + 4 * lane);
      vv = *(const GAS u32x2*)((const gu16*)args[4] + g * HD + 4 * (lane & 31));
      const float bias = (1.0f - lo16(((const gu16*)args[5])[g / HKV])) * -1000000000.0f;
      st = (lo16(kk[0]) * lo16(qq[0]) + hi16(kk[0]) * hi16(qq[0])) + (lo16(kk[1]) * lo16(qq[1]) + hi16(kk[1]) * hi16(qq[1]));
      st = (x16_sum(sum16(st)) * SCALE + bias) * LOG2E;
    }
    asm volatile("s_waitcnt vmcnt(0)" ::: "memory");
    f32x4 w4;
    do {
      __builtin_amdgcn_s_sleep(8);
      CFENCE();
      w4 = *reinterpret_cast<const f32x4*>(lds_m + (lane >> 5) * 4);
    } while (__builtin_amdgcn_uicmp(umax4(__builtin_bit_cast(u32x4, w4)), EMPTY, 32));
    CFENCE();
    STAMP(1)
    tb = __builtin_amdgcn_readfirstlane(__float_as_uint(lds_m[9]));
    const float M = fmaxf(fmaxf(w4[0], w4[1]), fmaxf(w4[2], w4[3]));
#pragma unroll
    for (int c = 0; c < 4; ++c) w4[c] = __builtin_amdgcn_exp2f(w4[c] - M);
    float L = 0.f;
#pragma unroll 1
    for (int k = 0; k < 8; ++k) {
      const f32x4 x = *reinterpret_cast<const f32x4*>(lds + (lane >> 5) * 1024 + k * 128 + (lane & 31) * 4);
      L += (__builtin_amdgcn_exp2f(fmaf(x[0], C2, -M)) + __builtin_amdgcn_exp2f(fmaf(x[1], C2, -M))) +
           (__builtin_amdgcn_exp2f(fmaf(x[2], C2, -M)) + __builtin_amdgcn_exp2f(fmaf(x[3], C2, -M)));
    }
    L = x16_sum(sum16(L));
#define REC(i) (*reinterpret_cast<const f32x4*>(lds_o + (i) * 2 * HD + 4 * lane))
    if (s != 0) {
      const u32x4 gm = {(lane & 1) ? tb : __float_as_uint(M), tag, __float_as_uint(L), tag};
      __builtin_amdgcn_raw_buffer_store_b128(gm, rw, (lane & 24) == 0 ? (lane >> 5) * 128 + (lane & 7) * 16 : OOB, WS_ML + bid * 256, AUX_SC1);
      STAMP(3)
      WG_BARRIER();
      STAMP(4)
      u32x4 go = __builtin_bit_cast(u32x4, (w4[0] * REC(0) + w4[1] * REC(1)) + (w4[2] * REC(2) + w4[3] * REC(3)));
#pragma unroll
      for (int c = 0; c < 4; ++c) go[c] = min(go[c], EMPTY - 1u);
      __builtin_amdgcn_raw_buffer_store_b128(go, rw, lane * 16, WS_O + bid * 1024, AUX_SC1);
    } else {
      NAP(NOW64() + 150 * 64);
      u32x4 a;
      for (;;) {
        CFENCE();
        a = __builtin_amdgcn_raw_buffer_load_b128(rw, (lane & 7) * 256 + (lane >> 5) * 128 + (lane & 8) * 2, WS_ML + bid * 256, AUX_SC1);
        if (((__builtin_amdgcn_uicmp(a[1], tag, 33) | __builtin_amdgcn_uicmp(a[3], tag, 33)) & 0xFEFEFEFEFEFEFEFEull) == 0) break;
        __builtin_amdgcn_s_sleep(16);
      }
      if (lane == 0) __hip_atomic_store((gu32*)(ws + WS_EP + g * 4352), tag, RLX_AGENT);
      const float mk = (lane & 7) == 0 ? M : __uint_as_float(a[0]), lk = (lane & 7) == 0 ? L : __uint_as_float(a[2]);
      const float Mx = max8(fmaxf(mk, st));
      const float wk = __builtin_amdgcn_exp2f(mk - Mx), wt = __builtin_amdgcn_exp2f(st - Mx);
      const float inv = 1.0f / (sum8(wk * lk) + wt), ck = wk * inv;
      w4 *= rowb<0>(ck);
      const float c1 = rowb<1>(ck), c2 = rowb<2>(ck), c3 = rowb<3>(ck), c4 = rowb<4>(ck), c5 = rowb<5>(ck), c6 = rowb<6>(ck), c7 = rowb<7>(ck);
      f32x4 o = rowb<0>(wt * inv) * f32x4{lo16(vv[0]), hi16(vv[0]), lo16(vv[1]), hi16(vv[1])};
      const float skew = max8((lane & 7) == 0 ? 0.f : (float)(int)(a[0] - tb));
      const unsigned te = tb + (HEAD + TOTAL + PDELAY) * 64 + __builtin_amdgcn_readlane((int)skew, 8);
      gu64* op = (gu64*)((gu16*)args[6] + g * 2 * HD + 4 * lane);
      STAMP(3)
      WG_BARRIER();
      STAMP(4)
      unsigned tp = NOW64() + POWN * 64;
      o += (w4[0] * REC(0) + w4[1] * REC(1)) + (w4[2] * REC(2) + w4[3] * REC(3));
      if ((int)(te - tp) > 0) tp = te;
      PACE(tp, dt);
      u32x4 po[NSPLIT];
      unsigned mx;
      do {
        CFENCE();
#pragma unroll
        for (int k = 1; k < NSPLIT; ++k) po[k] = __builtin_amdgcn_raw_buffer_load_b128(rw, lane * 16 + (k & 3) * 1024, WS_O + bid * 1024 + (k >> 2) * 4096, AUX_SC1);
        mx = umax4(po[1]);
#pragma unroll
        for (int k = 2; k < NSPLIT; ++k) mx = max(mx, umax4(po[k]));
      } while (__builtin_amdgcn_uicmp(mx, EMPTY, 32));
#define PO(k) __builtin_bit_cast(f32x4, po[k])
      o = ((o + c1 * PO(1)) + (c2 * PO(2) + c3 * PO(3))) + ((c4 * PO(4) + c5 * PO(5)) + (c6 * PO(6) + c7 * PO(7)));
      __hip_atomic_store(op, (unsigned long long)cvtpk(o[0], o[1]) | ((unsigned long long)cvtpk(o[2], o[3]) << 32), RLX_AGENT);
      if (DPROBE) { asm volatile("s_waitcnt vmcnt(0)" ::: "memory"); const unsigned tb0 = (unsigned)tb64 << 6, until = tb0 + 3000 * 64 + 5 * (NOW64() - tb0 - 1800 * 64); while ((int)(NOW64() - until) < 0) __builtin_amdgcn_s_sleep(1); }
    }
    STAMP(5)
  } else {
    if (INJC | INJR | INJS) { const unsigned dl = 25 + INJC + (INJR ? (((bid + (tb >> 18)) * 2654435761u) >> 16) % (unsigned)(INJR + 1) : 0u); while ((int)(NOW64() - tb) < (int)dl * 64) {} }
    const __amdgpu_buffer_rsrc_t rk = __builtin_amdgcn_make_buffer_rsrc((void*)args[1], 0, NGRP * CTX * HD * 2, 0x00020000);
    const __amdgpu_buffer_rsrc_t rv = __builtin_amdgcn_make_buffer_rsrc((void*)args[2], 0, NGRP * CTX * HD * 2, 0x00020000);
    unsigned tn;
    u32x4 buf[8], qf;
#pragma unroll
    for (int i = 0; i < 8; ++i) {
      if (i >= 5) PACEC(tn, dt, THR(i), THR2(i)); else if (i >= NSKIP) PACE(tn, dt);
      buf[i] = __builtin_amdgcn_raw_buffer_load_b128(rk, kvo, reg ^ LOFF(i), AUX_NT);
      SB();
      if (i == 0) { qf = *(const GAS u32x4*)((const gu8*)q + qo); SB(); }
      if (i == NSKIP - 1) {
        __builtin_amdgcn_s_barrier();
        tb += max((int)(NOW64() - tb) - KSLIP * 64, 0);
        tn = tb + t0 + NSKIP * dt;
        lds_m[9 + wave] = __uint_as_float(tb);
        SB();
      }
    }
    const int jj = (r & 3) + 4 * (r >> 3);
    unsigned msk[4];
#pragma unroll
    for (int c = 0; c < 4; ++c) msk[c] = (c == (jj >> 1)) ? ((jj & 1) ? 0xFFFF0000u : 0x0000FFFFu) : 0u;
    asm volatile("" : "+v"(msk[0]), "+v"(msk[1]), "+v"(msk[2]), "+v"(msk[3]), "+v"(mp));
    float* sp = lds + (((lane >> 2) & 1) * 4 + wave) * 256 + h * 8;
    float m = -INFINITY;
#pragma unroll 1
    for (int b = 1; b <= 8; ++b) {
      const __amdgpu_buffer_rsrc_t rs = __builtin_amdgcn_make_buffer_rsrc((void*)(b < 8 ? args[1] : args[2]), 0, NGRP * CTX * HD * 2, 0x00020000);
      const unsigned vo = b < 8 ? kvo : vvo, bo = BOFF(b);
      float S[8];
#pragma unroll
      for (int i = 0; i < 8; ++i) {
        const f32x4 z = {0.f, 0.f, 0.f, 0.f};
        const f32x4 d = __builtin_amdgcn_mfma_f32_16x16x32_bf16(__builtin_bit_cast(bf16x8, buf[i]), __builtin_bit_cast(bf16x8, qf), z, 0, 0, 0);
        S[i] = (dpp<0x00>(d[0]) + dpp<0x55>(d[1])) + (dpp<0xAA>(d[2]) + dpp<0xFF>(d[3]));
        m = fmaxf(m, S[i]);
        asm volatile("" : "+v"(m));
        if ((i & 3) == 3) *reinterpret_cast<f32x4*>(sp + (b - 1) * 32 + (i & 4)) = f32x4{S[i - 3], S[i - 2], S[i - 1], S[i]};
        SB();
        PACEC(tn, dt, THR(i), THR2(i));
        buf[i] = __builtin_amdgcn_raw_buffer_load_b128(rs, vo, bo ^ LOFF(i), AUX_NT);
        SB();
      }
    }
    f32x4 s0 = *reinterpret_cast<const f32x4*>(sp), s1 = *reinterpret_cast<const f32x4*>(sp + 4);
    const float mc = xh_max(m) * C2;
    CFENCE();
    *mp = __uint_as_float(min(__float_as_uint(mc), EMPTY - 1u));
    f32x4 acc = {0.f, 0.f, 0.f, 0.f};
#define PV(sc, i) do { const float p_ = __builtin_amdgcn_exp2f(fmaf(sc, C2, -mc)); const unsigned pp_ = cvtpk(p_, p_); \
      const u32x4 a_ = {pp_ & msk[0], pp_ & msk[1], pp_ & msk[2], pp_ & msk[3]}; \
      acc = __builtin_amdgcn_mfma_f32_16x16x32_bf16(__builtin_bit_cast(bf16x8, a_), __builtin_bit_cast(bf16x8, buf[i]), acc, 0, 0, 0); SB(); } while (0)
#pragma unroll 1
    for (int b = 1; b < 8; ++b) {
      const unsigned bo = BOFF(b);
      const int te = b == 7 ? 0 : (LAG2 + GR2) * 64;
#pragma unroll
      for (int i = 0; i < 8; ++i) {
        PV(i < 4 ? s0[i & 3] : s1[i & 3], i);
        if (i == 3) s0 = *reinterpret_cast<const f32x4*>(sp + b * 32);
        if (i == 7) s1 = *reinterpret_cast<const f32x4*>(sp + b * 32 + 4);
        if (i >= 8 - NFREE) PACEE(tn, dt, THR(i), te); else PACEC(tn, dt, THR(i), THR2(i));
        buf[i] = __builtin_amdgcn_raw_buffer_load_b128(rv, vvo, bo ^ LOFF(i), AUX_NT);
        SB();
      }
    }
#pragma unroll
    for (int i = 0; i < 8; ++i) PV(i < 4 ? s0[i & 3] : s1[i & 3], i);
    *reinterpret_cast<f32x4*>(lds_o + wave * 2 * HD + (h & 1) * HD + 8 * r + 4 * (h >> 1)) = acc;
    WG_BARRIER();
  }
  STAMP_OUT
}

extern "C" void kernel_launch(void* const* d_in, const int* in_sizes, int n_in, void* d_out, int out_size, void* d_ws, size_t ws_size,
                              hipStream_t stream) {
  tree_attn_decode<<<NGRP * NSPLIT, WAVES * 64, 0, stream>>>((const unsigned short*)d_in[0], (const unsigned short*)d_in[1], (const unsigned short*)d_in[2],
                                                             (const unsigned short*)d_in[3], (const unsigned short*)d_in[4], (const unsigned short*)d_in[5],
                                                             (unsigned short*)d_out, (unsigned char*)d_ws);
}
```

```cpp
#include <hip/hip_runtime.h>
#include <hip/hip_bf16.h>
#include <stdint.h>

typedef __attribute__((ext_vector_type(8))) __bf16 bf16x8;
typedef __attribute__((ext_vector_type(4))) float f32x4;
typedef __attribute__((ext_vector_type(2))) float f32x2;
typedef __attribute__((ext_vector_type(4))) unsigned u32x4;
typedef __attribute__((ext_vector_type(2))) unsigned u32x2;
typedef __attribute__((ext_vector_type(8))) unsigned long long u64x8;
#define GAS __attribute__((address_space(1)))
typedef GAS unsigned char gu8;
typedef GAS unsigned short gu16;
typedef GAS unsigned gu32;
typedef GAS unsigned long long gu64;
typedef __attribute__((address_space(3))) float lf32;

#define TOTAL 1710
#define HEAD 0
#define PDELAY 90
#define POWN 15
#define LAGL 24
#define LAG2 30
#define GR2 30
#define NSKIP 2
#define NFREE 4
#define DPROBE 0
#define INJC 0
#define INJR 0
#define INJS 0
#define PATC 0x40, 0x66f, 0x6d4, 0x4e5, 0x200, 0xe1, 0x2a5, 0x28, 0x2948, 0x1029, 0x33cd, 0x829, 0xc9d, 0x46c, 0x805, 0x78f
#define SPFV 0
#define KSLIP 36
#define STAMP(i)
#define STAMP_DECL
#define STAMP_OUT

constexpr int NB = 2, HQ = 32, HKV = 16, CTX = 8192, HD = 128;
constexpr int NGRP = NB * HKV;
constexpr int NSPLIT = 8, WAVES = 5;
constexpr float SCALE = 0.088388347648318440f, LOG2E = 1.4426950408889634f;
constexpr float C2 = SCALE * LOG2E;
constexpr int WS_O = 0, WS_ML = 262144, WS_EP = WS_ML + 65536, WS_END = WS_EP + NGRP * 4352;
constexpr int AUX_NT = 2, AUX_SC1 = 16;
constexpr unsigned PCOL[16] = {PATC};
constexpr unsigned OOB = 0x80000000u, EMPTY = 0xFFFFFFFFu;

__device__ __forceinline__ unsigned cvtpk(float lo, float hi) {
  typedef __attribute__((ext_vector_type(2))) __bf16 bf16x2;
  f32x2 v = {lo, hi};
  return __builtin_bit_cast(unsigned, __builtin_convertvector(v, bf16x2));
}
template <int CTRL> __device__ __forceinline__ float dpp(float x) {
  return __builtin_bit_cast(float, __builtin_amdgcn_mov_dpp(__builtin_bit_cast(int, x), CTRL, 0xf, 0xf, true));
}
template <int I> __device__ __forceinline__ float rowb(float x) { return dpp<0x150 + I>(x); }
__device__ __forceinline__ float x16_sum(float x) {
  auto s = __builtin_amdgcn_permlane16_swap(__float_as_uint(x), __float_as_uint(x), false, false);
  return __uint_as_float(s[0]) + __uint_as_float(s[1]);
}
__device__ __forceinline__ float xh_max(float x) {
  auto s = __builtin_amdgcn_permlane16_swap(__float_as_uint(x), __float_as_uint(x), false, false);
  x = fmaxf(__uint_as_float(s[0]), __uint_as_float(s[1]));
  auto t = __builtin_amdgcn_permlane32_swap(__float_as_uint(x), __float_as_uint(x), false, false);
  return fmaxf(__uint_as_float(t[0]), __uint_as_float(t[1]));
}
__device__ __forceinline__ float max8(float x) { x = fmaxf(x, dpp<0xB1>(x)); x = fmaxf(x, dpp<0x4E>(x)); return fmaxf(x, dpp<0x141>(x)); }
__device__ __forceinline__ float sum8(float x) { x += dpp<0xB1>(x); x += dpp<0x4E>(x); return x + dpp<0x141>(x); }
__device__ __forceinline__ float sum16(float x) { x = sum8(x); return x + dpp<0x128>(x); }
__device__ __forceinline__ float lo16(unsigned x) { return __uint_as_float(x << 16); }
__device__ __forceinline__ float hi16(unsigned x) { return __uint_as_float(x & 0xFFFF0000u); }
__device__ __forceinline__ unsigned umax4(u32x4 v) { return max(max(v[0], v[1]), max(v[2], v[3])); }
#define RLX_AGENT __ATOMIC_RELAXED, __HIP_MEMORY_SCOPE_AGENT
#define RLX_WG __ATOMIC_RELAXED, __HIP_MEMORY_SCOPE_WORKGROUP
#define CFENCE() asm volatile("" ::: "memory")
#define SB() __builtin_amdgcn_sched_barrier(0)
#define WG_BARRIER() asm volatile("s_waitcnt lgkmcnt(0)\n\ts_barrier" ::: "memory")
#define PACE_TXT "1:\n\ts_memrealtime vcc\n\ts_waitcnt lgkmcnt(0)\n\ts_lshl_b32 vcc_lo, vcc_lo, 6\n\t" \
    "s_sub_i32 vcc_lo, vcc_lo, %0\n\ts_cmp_lt_i32 vcc_lo, 0\n\ts_cbranch_scc1 1b\n\ts_add_i32 %0, %0, %1"
#define PACE(t, d) asm volatile(PACE_TXT : "+s"(t) : "s"(d) : "vcc", "scc", "memory")
#define PACEC(t, d, thr, thr2) asm volatile(PACE_TXT "\n\ts_cmpk_ge_i32 vcc_lo, %3\n\ts_cbranch_scc1 3f\n\ts_cmpk_ge_i32 vcc_lo, %2\n\ts_cbranch_scc1 2f\n\t" \
    "s_waitcnt vmcnt(4)\n2:\n\ts_waitcnt vmcnt(5)\n3:\n\ts_waitcnt vmcnt(6)" : "+s"(t) : "s"(d), "i"(thr), "i"(thr2) : "vcc", "scc", "memory")
#define PACEE(t, d, thr, thr2) asm volatile(PACE_TXT "\n\ts_cmp_ge_i32 vcc_lo, %3\n\ts_cbranch_scc1 3f\n\ts_cmpk_ge_i32 vcc_lo, %2\n\ts_cbranch_scc1 2f\n\t" \
    "s_waitcnt vmcnt(4)\n2:\n\ts_waitcnt vmcnt(5)\n3:\n\ts_waitcnt vmcnt(6)" : "+s"(t) : "s"(d), "i"(thr), "s"(thr2) : "vcc", "scc", "memory")
#define ORD(i) (((i) & 3) == 0 ? 1 : ((i) & 3) == 1 ? 3 : ((i) & 3) == 2 ? 2 : 4)
#define THR(i) (LAGL * 16 * ORD(i))
#define THR2(i) (LAG2 * 64 + GR2 * 16 * ORD(i))
#define NOW64() ((unsigned)__builtin_amdgcn_s_memrealtime() << 6)
#define NAP(t) do { const unsigned t_ = (t); while ((int)(NOW64() - t_) < 0) __builtin_amdgcn_s_sleep(4); } while (0)

__global__ void __launch_bounds__(WAVES * 64)
tree_attn_decode(const unsigned short*, const unsigned short*, const unsigned short*, const unsigned short*, const unsigned short*,
                 const unsigned short*, unsigned short*, unsigned char*) {
  u64x8 args;
  unsigned long long tb64;
  asm volatile("s_load_dwordx16 %0, %2, 0x0\n\ts_memrealtime %1" : "=&s"(args), "=&s"(tb64) : "s"(__builtin_amdgcn_kernarg_segment_ptr()));
  SB();
  __shared__ __attribute__((aligned(16))) float lds[2048 + 1024 + 16];
  float* lds_o = lds + 2048;
  float* lds_m = lds_o + 1024;
  STAMP_DECL
  STAMP(0)
  const int bid = blockIdx.x, g = bid >> 3, s = bid & 7;
  const int tid = threadIdx.x, lane = tid & 63, wave = __builtin_amdgcn_readfirstlane(tid >> 6);
  const int r = lane & 15, h = lane >> 4;
  if (wave >= 4) {
    unsigned long long pc;
    unsigned j0, j1, j2, j3;
    asm volatile("s_getpc_b64 %0" : "=s"(pc));
    asm volatile("s_load_dword %0, %4, 0x0\n\ts_load_dword %1, %4, 0x40\n\ts_load_dword %2, %4, 0x80\n\ts_load_dword %3, %4, 0xc0\n\t"
                 "s_barrier\n\ts_waitcnt lgkmcnt(0)"
                 : "=&s"(j0), "=&s"(j1), "=&s"(j2), "=&s"(j3) : "s"((pc & ~255ull) + g * 256) : "memory");
  }
  const unsigned kvo = (r >> 2) * 256 + h * 64 + (r & 3) * 16, vvo = lane * 16;
  unsigned c0 = 0;
#pragma unroll
  for (int b = 0; b < 2; ++b) c0 ^= ((wave >> b) & 1) ? PCOL[6 + b] : 0u;
#pragma unroll
  for (int b = 0; b < 8; ++b) c0 ^= ((bid >> b) & 1) ? PCOL[8 + b] : 0u;
  const unsigned reg = ((c0 & 0x7FF) << 10) | (g << 21);
#define LOFF(i) (((((i) & 1) ? PCOL[0] : 0u) ^ (((i) & 2) ? PCOL[1] : 0u) ^ (((i) & 4) ? PCOL[2] : 0u)) << 10)
#define BOFF(b) (reg ^ (((((b) & 1) ? PCOL[3] : 0u) ^ (((b) & 2) ? PCOL[4] : 0u) ^ (((b) & 4) ? PCOL[5] : 0u)) << 10))
  const unsigned t0 = ((wave * 8 + (c0 >> 11)) * TOTAL >> 6) + HEAD * 64, dt = TOTAL / 2;
  const unsigned qo = (g * 2 + ((r >> 2) & 1)) * 256 + (4 * h + (r & 3)) * 16;
  lf32* mp = (lf32*)(lds_m + (lane & 4) + (wave & 3) + (wave & 4) * 2);
  *mp = __uint_as_float(EMPTY);
  asm volatile("" :: "s"(t0), "s"(reg), "v"(kvo), "v"(qo) : "memory");
  SB();
  asm volatile("s_waitcnt lgkmcnt(0)" : "+s"(args), "+s"(tb64));
  const gu16* q = (const gu16*)args[0];
  gu8* ws = (gu8*)args[7];
  unsigned tb = (unsigned)tb64 << 6;
  if (INJS) tb += ((((s + 8 * (tb >> 18)) * 2654435761u) >> 16) % (unsigned)(INJS + 1)) * 64;
  if (wave >= 4) {
    const __amdgpu_buffer_rsrc_t rw = __builtin_amdgcn_make_buffer_rsrc((void*)ws, 0, WS_END, 0x00020000);
    NAP(tb + 250 * 64);
    if (SPFV) { unsigned j0, j1; asm volatile("s_load_dword %0, %2, 0x0\n\ts_load_dword %1, %3, 0x0\n\ts_waitcnt lgkmcnt(0)" : "=&s"(j0), "=&s"(j1) : "s"(args[2] + reg), "s"(args[7] + bid * 1024) : "memory"); }
    if (SPFV == 2) { const unsigned jv = *(const volatile GAS unsigned*)(args[2] + reg); asm volatile("" :: "v"(jv)); }
    const unsigned tag = __hip_atomic_load((gu32*)(ws + WS_EP + g * 4352), RLX_AGENT) + 1u;
    __builtin_amdgcn_raw_buffer_store_b128(u32x4{EMPTY, EMPTY, EMPTY, EMPTY}, rw, lane * 16, WS_O + bid * 1024, AUX_SC1);
    float st = 0.f;
    u32x2 vv = {0u, 0u};
    if (s == 0) {
      const u32x2 kk = *(const GAS u32x2*)((const gu16*)args[3] + g * HD + 4 * (lane & 31));
      const u32x2 qq = *(const GAS u32x2*)(q + g * 2 * HD + 4 * lane);
      vv = *(const GAS u32x2*)((const gu16*)args[4] + g * HD + 4 * (lane & 31));
      const float bias = (1.0f - lo16(((const gu16*)args[5])[g / HKV])) * -1000000000.0f;
      st = (lo16(kk[0]) * lo16(qq[0]) + hi16(kk[0]) * hi16(qq[0])) + (lo16(kk[1]) * lo16(qq[1]) + hi16(kk[1]) * hi16(qq[1]));
      st = (x16_sum(sum16(st)) * SCALE + bias) * LOG2E;
    }
    asm volatile("s_waitcnt vmcnt(0)" ::: "memory");
    f32x4 w4;
    do {
      __builtin_amdgcn_s_sleep(8);
      CFENCE();
      w4 = *reinterpret_cast<const f32x4*>(lds_m + (lane >> 5) * 4);
    } while (__builtin_amdgcn_uicmp(umax4(__builtin_bit_cast(u32x4, w4)), EMPTY, 32));
    CFENCE();
    STAMP(1)
    tb = __builtin_amdgcn_readfirstlane(__float_as_uint(lds_m[9]));
    const float M = fmaxf(fmaxf(w4[0], w4[1]), fmaxf(w4[2], w4[3]));
#pragma unroll
    for (int c = 0; c < 4; ++c) w4[c] = __builtin_amdgcn_exp2f(w4[c] - M);
    float L = 0.f;
#pragma unroll 1
    for (int k = 0; k < 8; ++k) {
      const f32x4 x = *reinterpret_cast<const f32x4*>(lds + (lane >> 5) * 1024 + k * 128 + (lane & 31) * 4);
      L += (__builtin_amdgcn_exp2f(fmaf(x[0], C2, -M)) + __builtin_amdgcn_exp2f(fmaf(x[1], C2, -M))) +
           (__builtin_amdgcn_exp2f(fmaf(x[2], C2, -M)) + __builtin_amdgcn_exp2f(fmaf(x[3], C2, -M)));
    }
    L = x16_sum(sum16(L));
#define REC(i) (*reinterpret_cast<const f32x4*>(lds_o + (i) * 2 * HD + 4 * lane))
    if (s != 0) {
      const u32x4 gm = {(lane & 1) ? tb : __float_as_uint(M), tag, __float_as_uint(L), tag};
      __builtin_amdgcn_raw_buffer_store_b128(gm, rw, (lane & 24) == 0 ? (lane >> 5) * 128 + (lane & 7) * 16 : OOB, WS_ML + bid * 256, AUX_SC1);
      STAMP(3)
      WG_BARRIER();
      STAMP(4)
      u32x4 go = __builtin_bit_cast(u32x4, (w4[0] * REC(0) + w4[1] * REC(1)) + (w4[2] * REC(2) + w4[3] * REC(3)));
#pragma unroll
      for (int c = 0; c < 4; ++c) go[c] = min(go[c], EMPTY - 1u);
      __builtin_amdgcn_raw_buffer_store_b128(go, rw, lane * 16, WS_O + bid * 1024, AUX_SC1);
    } else {
      NAP(NOW64() + 150 * 64);
      u32x4 a;
      for (;;) {
        CFENCE();
        a = __builtin_amdgcn_raw_buffer_load_b128(rw, (lane & 7) * 256 + (lane >> 5) * 128 + (lane & 8) * 2, WS_ML + bid * 256, AUX_SC1);
        if (((__builtin_amdgcn_uicmp(a[1], tag, 33) | __builtin_amdgcn_uicmp(a[3], tag, 33)) & 0xFEFEFEFEFEFEFEFEull) == 0) break;
        __builtin_amdgcn_s_sleep(16);
      }
      if (lane == 0) __hip_atomic_store((gu32*)(ws + WS_EP + g * 4352), tag, RLX_AGENT);
      const float mk = (lane & 7) == 0 ? M : __uint_as_float(a[0]), lk = (lane & 7) == 0 ? L : __uint_as_float(a[2]);
      const float Mx = max8(fmaxf(mk, st));
      const float wk = __builtin_amdgcn_exp2f(mk - Mx), wt = __builtin_amdgcn_exp2f(st - Mx);
      const float inv = 1.0f / (sum8(wk * lk) + wt), ck = wk * inv;
      w4 *= rowb<0>(ck);
      const float c1 = rowb<1>(ck), c2 = rowb<2>(ck), c3 = rowb<3>(ck), c4 = rowb<4>(ck), c5 = rowb<5>(ck), c6 = rowb<6>(ck), c7 = rowb<7>(ck);
      f32x4 o = rowb<0>(wt * inv) * f32x4{lo16(vv[0]), hi16(vv[0]), lo16(vv[1]), hi16(vv[1])};
      const float skew = max8((lane & 7) == 0 ? 0.f : (float)(int)(a[0] - tb));
      const unsigned te = tb + (HEAD + TOTAL + PDELAY) * 64 + __builtin_amdgcn_readlane((int)skew, 8);
      gu64* op = (gu64*)((gu16*)args[6] + g * 2 * HD + 4 * lane);
      STAMP(3)
      WG_BARRIER();
      STAMP(4)
      unsigned tp = NOW64() + POWN * 64;
      o += (w4[0] * REC(0) + w4[1] * REC(1)) + (w4[2] * REC(2) + w4[3] * REC(3));
      if ((int)(te - tp) > 0) tp = te;
      PACE(tp, dt);
      u32x4 po[NSPLIT];
      unsigned mx;
      do {
        CFENCE();
#pragma unroll
        for (int k = 1; k < NSPLIT; ++k) po[k] = __builtin_amdgcn_raw_buffer_load_b128(rw, lane * 16 + (k & 3) * 1024, WS_O + bid * 1024 + (k >> 2) * 4096, AUX_SC1);
        mx = umax4(po[1]);
#pragma unroll
        for (int k = 2; k < NSPLIT; ++k) mx = max(mx, umax4(po[k]));
      } while (__builtin_amdgcn_uicmp(mx, EMPTY, 32));
#define PO(k) __builtin_bit_cast(f32x4, po[k])
      o = ((o + c1 * PO(1)) + (c2 * PO(2) + c3 * PO(3))) + ((c4 * PO(4) + c5 * PO(5)) + (c6 * PO(6) + c7 * PO(7)));
      __hip_atomic_store(op, (unsigned long long)cvtpk(o[0], o[1]) | ((unsigned long long)cvtpk(o[2], o[3]) << 32), RLX_AGENT);
      if (DPROBE) { asm volatile("s_waitcnt vmcnt(0)" ::: "memory"); const unsigned tb0 = (unsigned)tb64 << 6, until = tb0 + 3000 * 64 + 5 * (NOW64() - tb0 - 1800 * 64); while ((int)(NOW64() - until) < 0) __builtin_amdgcn_s_sleep(1); }
    }
    STAMP(5)
  } else {
    if (INJC | INJR | INJS) { const unsigned dl = 25 + INJC + (INJR ? (((bid + (tb >> 18)) * 2654435761u) >> 16) % (unsigned)(INJR + 1) : 0u); while ((int)(NOW64() - tb) < (int)dl * 64) {} }
    const __amdgpu_buffer_rsrc_t rk = __builtin_amdgcn_make_buffer_rsrc((void*)args[1], 0, NGRP * CTX * HD * 2, 0x00020000);
    const __amdgpu_buffer_rsrc_t rv = __builtin_amdgcn_make_buffer_rsrc((void*)args[2], 0, NGRP * CTX * HD * 2, 0x00020000);
    unsigned tn;
    u32x4 buf[8], qf;
#pragma unroll
    for (int i = 0; i < 8; ++i) {
      if (i >= 5) PACEC(tn, dt, THR(i), THR2(i)); else if (i >= NSKIP) PACE(tn, dt);
      buf[i] = __builtin_amdgcn_raw_buffer_load_b128(rk, kvo, reg ^ LOFF(i), AUX_NT);
      SB();
      if (i == 0) { qf = *(const GAS u32x4*)((const gu8*)q + qo); SB(); }
      if (i == NSKIP - 1) {
        __builtin_amdgcn_s_barrier();
        tb += max((int)(NOW64() - tb) - KSLIP * 64, 0);
        tn = tb + t0 + NSKIP * dt;
        lds_m[9 + wave] = __uint_as_float(tb);
        SB();
      }
    }
    const int jj = (r & 3) + 4 * (r >> 3);
    unsigned msk[4];
#pragma unroll
    for (int c = 0; c < 4; ++c) msk[c] = (c == (jj >> 1)) ? ((jj & 1) ? 0xFFFF0000u : 0x0000FFFFu) : 0u;
    asm volatile("" : "+v"(msk[0]), "+v"(msk[1]), "+v"(msk[2]), "+v"(msk[3]), "+v"(mp));
    float* sp = lds + (((lane >> 2) & 1) * 4 + wave) * 256 + h * 8;
    float m = -INFINITY;
#pragma unroll 1
    for (int b = 1; b <= 8; ++b) {
      const __amdgpu_buffer_rsrc_t rs = __builtin_amdgcn_make_buffer_rsrc((void*)(b < 8 ? args[1] : args[2]), 0, NGRP * CTX * HD * 2, 0x00020000);
      const unsigned vo = b < 8 ? kvo : vvo, bo = BOFF(b);
      float S[8];
#pragma unroll
      for (int i = 0; i < 8; ++i) {
        const f32x4 z = {0.f, 0.f, 0.f, 0.f};
        const f32x4 d = __builtin_amdgcn_mfma_f32_16x16x32_bf16(__builtin_bit_cast(bf16x8, buf[i]), __builtin_bit_cast(bf16x8, qf), z, 0, 0, 0);
        S[i] = (dpp<0x00>(d[0]) + dpp<0x55>(d[1])) + (dpp<0xAA>(d[2]) + dpp<0xFF>(d[3]));
        m = fmaxf(m, S[i]);
        asm volatile("" : "+v"(m));
        if ((i & 3) == 3) *reinterpret_cast<f32x4*>(sp + (b - 1) * 32 + (i & 4)) = f32x4{S[i - 3], S[i - 2], S[i - 1], S[i]};
        SB();
        PACEC(tn, dt, THR(i), THR2(i));
        buf[i] = __builtin_amdgcn_raw_buffer_load_b128(rs, vo, bo ^ LOFF(i), AUX_NT);
        SB();
      }
    }
    f32x4 s0 = *reinterpret_cast<const f32x4*>(sp), s1 = *reinterpret_cast<const f32x4*>(sp + 4);
    const float mc = xh_max(m) * C2;
    CFENCE();
    *mp = __uint_as_float(min(__float_as_uint(mc), EMPTY - 1u));
    f32x4 acc = {0.f, 0.f, 0.f, 0.f};
#define PV(sc, i) do { const float p_ = __builtin_amdgcn_exp2f(fmaf(sc, C2, -mc)); const unsigned pp_ = cvtpk(p_, p_); \
      const u32x4 a_ = {pp_ & msk[0], pp_ & msk[1], pp_ & msk[2], pp_ & msk[3]}; \
      acc = __builtin_amdgcn_mfma_f32_16x16x32_bf16(__builtin_bit_cast(bf16x8, a_), __builtin_bit_cast(bf16x8, buf[i]), acc, 0, 0, 0); SB(); } while (0)
#pragma unroll 1
    for (int b = 1; b < 8; ++b) {
      const unsigned bo = BOFF(b);
      const int te = b == 7 ? 0 : (LAG2 + GR2) * 64;
#pragma unroll
      for (int i = 0; i < 8; ++i) {
        PV(i < 4 ? s0[i & 3] : s1[i & 3], i);
        if (i == 3) s0 = *reinterpret_cast<const f32x4*>(sp + b * 32);
        if (i == 7) s1 = *reinterpret_cast<const f32x4*>(sp + b * 32 + 4);
        if (i >= 8 - NFREE) PACEE(tn, dt, THR(i), te); else PACEC(tn, dt, THR(i), THR2(i));
        buf[i] = __builtin_amdgcn_raw_buffer_load_b128(rv, vvo, bo ^ LOFF(i), AUX_NT);
        SB();
      }
    }
#pragma unroll
    for (int i = 0; i < 8; ++i) PV(i < 4 ? s0[i & 3] : s1[i & 3], i);
    *reinterpret_cast<f32x4*>(lds_o + wave * 2 * HD + (h & 1) * HD + 8 * r + 4 * (h >> 1)) = acc;
    WG_BARRIER();
  }
  STAMP_OUT
}

extern "C" void kernel_launch(void* const* d_in, const int* in_sizes, int n_in, void* d_out, int out_size, void* d_ws, size_t ws_size,
                              hipStream_t stream) {
  tree_attn_decode<<<NGRP * NSPLIT, WAVES * 64, 0, stream>>>((const unsigned short*)d_in[0], (const unsigned short*)d_in[1], (const unsigned short*)d_in[2],
                                                             (const unsigned short*)d_in[3], (const unsigned short*)d_in[4], (const unsigned short*)d_in[5],
                                                             (unsigned short*)d_out, (unsigned char*)d_ws);
}
```
